```python
import torch

def _softmax(x, axis=-1):
    return torch.softmax(x, dim=axis)
def _gelu(x):
    return torch.nn.functional.gelu(x, approximate="tanh")
def _silu(x):
    return torch.nn.functional.silu(x)
def _sigmoid(x):
    return torch.sigmoid(x)
def _layernorm(x, axis=-1, eps=1e-5):
    m = x.mean(dim=axis, keepdim=True)
    v = x.var(dim=axis, keepdim=True, unbiased=False)
    return (x - m) / torch.sqrt(v + eps)
def _scatter_add(tgt, idx, src, axis=0):
    return tgt.clone().index_add_(axis, idx, src)
def _segment_sum(src, seg, n):
    out = torch.zeros((n,) + src.shape[1:], dtype=src.dtype)
    return out.index_add_(0, seg, src)
def _pad(x, pw):
    flat = tuple(v for pair in reversed(pw) for v in pair)
    return torch.nn.functional.pad(x, flat)

kernel_name = 'concat_heads__mha_block_cached__gqa_block_cached__near_ridge'

def setup_inputs():

    _w = (lambda a: (lambda t: t.to(torch.bfloat16) if t.is_floating_point() else t)(torch.as_tensor(a)))
    def _A_setup():
        B, Sq, D = 32, 48, 448
        H, Sk, Dh = 16, 48, 256
        return {
            "x": _w((lambda *s: torch.randn(*s, dtype=torch.bfloat16) * 0.8)(B, Sq, D)),
            "k_cache": _w((lambda *s: torch.randn(*s, dtype=torch.bfloat16) * 0.8)(B, H, Sk, Dh)),
            "v_cache": _w((lambda *s: torch.randn(*s, dtype=torch.bfloat16) * 0.05)(B, H, Sk, Dh)),
            "wq": _w((lambda *s: torch.randn(*s, dtype=torch.bfloat16) * 0.8)(D, H * Dh) * (D ** -0.5)),
            "wo": _w((lambda *s: torch.randn(*s, dtype=torch.bfloat16) * 0.05)(H * Dh, D) * ((H * Dh) ** -0.5)),
        }
    _A_in = _A_setup()

    def _B_setup():
        B, Sq, D = 32, 48, 448
        Hq, Hkv, Sk, Dh = 16, 16, 48, 256
        assert Hq % Hkv == 0, (Hq, Hkv)
        return {
            "x": _w((lambda *s: torch.randn(*s, dtype=torch.bfloat16) * 0.8)(B, Sq, D)),
            "k_cache": _w((lambda *s: torch.randn(*s, dtype=torch.bfloat16) * 0.8)(B, Hkv, Sk, Dh)),
            "v_cache": _w((lambda *s: torch.randn(*s, dtype=torch.bfloat16) * 0.05)(B, Hkv, Sk, Dh)),
            "wq": _w((lambda *s: torch.randn(*s, dtype=torch.bfloat16) * 0.8)(D, Hq * Dh) * (D ** -0.5)),
            "wo": _w((lambda *s: torch.randn(*s, dtype=torch.bfloat16) * 0.05)(Hq * Dh, D) * ((Hq * Dh) ** -0.5)),
        }
    _B_in = _B_setup()

    return {
        "A_x": _A_in["x"],
        "A_k_cache": _A_in["k_cache"],
        "A_v_cache": _A_in["v_cache"],
        "A_wq": _A_in["wq"],
        "A_wo": _A_in["wo"],
        "B_x": _B_in["x"],
        "B_k_cache": _B_in["k_cache"],
        "B_v_cache": _B_in["v_cache"],
        "B_wq": _B_in["wq"],
        "B_wo": _B_in["wo"],
    }

def reference(A_x, A_k_cache, A_v_cache, A_wq, A_wo, B_x, B_k_cache, B_v_cache, B_wq, B_wo):
    def _A_ref(x, k_cache, v_cache, wq, wo):
        B, Sq, D = x.shape
        H, Dh = 16, 256
        q = (lambda x, *d: x.permute(*d))((x @ wq).reshape(B, Sq, H, Dh), 0, 2, 1, 3)
        scores = torch.einsum("bhqd,bhkd->bhqk", q, k_cache) * (Dh ** -0.5)
        p = _softmax(scores, axis=-1)
        o = torch.einsum("bhqk,bhkd->bhqd", p, v_cache)
        return (lambda x, *d: x.permute(*d))(o, 0, 2, 1, 3).reshape(B, Sq, H * Dh) @ wo

    def _B_ref(x, k_cache, v_cache, wq, wo):
        B, Sq, D = x.shape
        Hq, Dh = 16, 256
        Hkv = k_cache.shape[1]
        rep = Hq // Hkv
        q = (lambda x, *d: x.permute(*d))((x @ wq).reshape(B, Sq, Hq, Dh), 0, 2, 1, 3)
        k = (lambda x, n, axis: torch.repeat_interleave(x, n, dim=axis))(k_cache, rep, axis=1)
        v = (lambda x, n, axis: torch.repeat_interleave(x, n, dim=axis))(v_cache, rep, axis=1)
        scores = torch.einsum("bhqd,bhkd->bhqk", q, k) * (Dh ** -0.5)
        p = _softmax(scores, axis=-1)
        o = torch.einsum("bhqk,bhkd->bhqd", p, v)
        return (lambda x, *d: x.permute(*d))(o, 0, 2, 1, 3).reshape(B, Sq, Hq * Dh) @ wo

    a = _A_ref(A_x, A_k_cache, A_v_cache, A_wq, A_wo)
    b = _B_ref(B_x, B_k_cache, B_v_cache, B_wq, B_wo)
    return torch.concatenate([a, b], axis=-1)
```

```cpp
#include <hip/hip_runtime.h>
#include <hip/hip_bf16.h>
typedef unsigned u32x4 __attribute__((ext_vector_type(4)));
typedef short bf16x8 __attribute__((ext_vector_type(8)));
typedef float f32x4 __attribute__((ext_vector_type(4)));
typedef unsigned short u16;
__device__ __forceinline__ u16 f2bf(float f){ __hip_bfloat16 b=__float2bfloat16(f); return *(u16*)&b; }
__device__ __forceinline__ float bf2f(u16 u){ return __uint_as_float((unsigned)u<<16); }

__global__ __launch_bounds__(256) void gemm(const u16* A0,const u16* A1,const u16* B0,const u16* B1,u16* C0,u16* C1,int K,int lda,int ldb,int ldc){
  const u16* A = blockIdx.z? A1:A0; const u16* B=blockIdx.z?B1:B0; u16* C=blockIdx.z?C1:C0;
  __shared__ __attribute__((aligned(16))) u16 sA[64][40];
  __shared__ __attribute__((aligned(16))) u16 sB[64][40];
  int tid=threadIdx.x, lane=tid&63, w=tid>>6;
  int m0=blockIdx.y*64, n0=blockIdx.x*64;
  f32x4 acc[4]={};
  for(int k0=0;k0<K;k0+=32){
    { int r=tid>>2,c=(tid&3)*8; *(u32x4*)&sA[r][c]=*(const u32x4*)&A[(size_t)(m0+r)*lda+k0+c]; }
    { int r=tid>>3,c=(tid&7)*8; u32x4 v=*(const u32x4*)&B[(size_t)(k0+r)*ldb+n0+c]; u16* pv=(u16*)&v;
      for(int j=0;j<8;j++) sB[c+j][r]=pv[j]; }
    __syncthreads();
    bf16x8 a=*(bf16x8*)&sA[w*16+(lane&15)][(lane>>4)*8];
    for(int n=0;n<4;n++){ bf16x8 b=*(bf16x8*)&sB[n*16+(lane&15)][(lane>>4)*8];
      acc[n]=__builtin_amdgcn_mfma_f32_16x16x32_bf16(a,b,acc[n],0,0,0);}
    __syncthreads();
  }
  for(int n=0;n<4;n++)for(int j=0;j<4;j++) C[(size_t)(m0+w*16+(lane>>4)*4+j)*ldc+n0+n*16+(lane&15)]=f2bf(acc[n][j]);
}
__global__ __launch_bounds__(256) void attn(const u16* Q0,const u16* Q1,const u16* K0,const u16* K1,const u16* V0,const u16* V1,u16* O0,u16* O1){
  int br=blockIdx.y; int bh=blockIdx.x; int b=bh>>4,h=bh&15;
  const u16* Q=(br?Q1:Q0)+(size_t)b*48*4096+h*256; const u16* Kp=(br?K1:K0)+(size_t)bh*48*256; const u16* Vp=(br?V1:V0)+(size_t)bh*48*256;
  u16* O=(br?O1:O0)+(size_t)b*48*4096+h*256;
  __shared__ float sS[48][49];
  __shared__ __attribute__((aligned(16))) u16 sV[48][264];
  int tid=threadIdx.x,lane=tid&63,w=tid>>6;
  for(int i=tid;i<48*32;i+=256){int r=i>>5,c=(i&31)*8; *(u32x4*)&sV[r][c]=*(const u32x4*)&Vp[r*256+c];}
  if(w<3){
    f32x4 acc[3]={};
    for(int k=0;k<8;k++){
      bf16x8 a=*(const bf16x8*)&Q[(size_t)(w*16+(lane&15))*4096+k*32+(lane>>4)*8];
      for(int n=0;n<3;n++){ bf16x8 bb=*(const bf16x8*)&Kp[(n*16+(lane&15))*256+k*32+(lane>>4)*8];
        acc[n]=__builtin_amdgcn_mfma_f32_16x16x32_bf16(a,bb,acc[n],0,0,0);}
    }
    for(int n=0;n<3;n++)for(int j=0;j<4;j++) sS[w*16+(lane>>4)*4+j][n*16+(lane&15)]=acc[n][j]*0.0625f;
  }
  __syncthreads();
  if(tid<48){ float m=-1e30f; for(int j=0;j<48;j++) m=fmaxf(m,sS[tid][j]); float s=0; for(int j=0;j<48;j++){float e=__expf(sS[tid][j]-m); sS[tid][j]=e; s+=e;}
    float r=1.f/s; for(int j=0;j<48;j++) sS[tid][j]=bf2f(f2bf(sS[tid][j]*r)); }
  __syncthreads();
  { int d=tid;
    for(int q=0;q<48;q+=4){ float a0=0,a1=0,a2=0,a3=0;
      for(int j=0;j<48;j++){ float v=bf2f(sV[j][d]); a0+=sS[q][j]*v; a1+=sS[q+1][j]*v; a2+=sS[q+2][j]*v; a3+=sS[q+3][j]*v;}
      O[(size_t)q*4096+d]=f2bf(a0); O[(size_t)(q+1)*4096+d]=f2bf(a1); O[(size_t)(q+2)*4096+d]=f2bf(a2); O[(size_t)(q+3)*4096+d]=f2bf(a3);} }
}
extern "C" void kernel_launch(void* const* d_in, const int* in_sizes, int n_in, void* d_out, int out_size, void* d_ws, size_t ws_size, hipStream_t stream) {
  const u16* const* in=(const u16* const*)d_in;
  u16* ws=(u16*)d_ws; size_t QS=(size_t)1536*4096;
  u16* Q0=ws,*Q1=ws+QS,*O0=ws+2*QS,*O1=ws+3*QS;
  gemm<<<dim3(64,24,2),256,0,stream>>>(in[0],in[5],in[3],in[8],Q0,Q1,448,448,4096,4096);
  attn<<<dim3(512,2),256,0,stream>>>(Q0,Q1,in[1],in[6],in[2],in[7],O0,O1);
  gemm<<<dim3(7,24,2),256,0,stream>>>(O0,O1,in[4],in[9],(u16*)d_out,(u16*)d_out+448,4096,4096,448,896);
}
```

```cpp
#define STREAM 1
#define AMP 20
#define IDX 9
#define STRIDE 225280
#include <hip/hip_runtime.h>
#include <hip/hip_bf16.h>
typedef unsigned u32x4 __attribute__((ext_vector_type(4)));
typedef short bf16x8 __attribute__((ext_vector_type(8)));
typedef float f32x4 __attribute__((ext_vector_type(4)));
typedef unsigned short u16;
__device__ __forceinline__ u16 f2bf(float f){ __hip_bfloat16 b=__float2bfloat16(f); return *(u16*)&b; }
__device__ __forceinline__ float bf2f(u16 u){ return __uint_as_float((unsigned)u<<16); }

__global__ __launch_bounds__(256) void gemm(const u16* A0,const u16* A1,const u16* B0,const u16* B1,u16* C0,u16* C1,int K,int lda,int ldb,int ldc){
  const u16* A = blockIdx.z? A1:A0; const u16* B=blockIdx.z?B1:B0; u16* C=blockIdx.z?C1:C0;
  __shared__ __attribute__((aligned(16))) u16 sA[64][40];
  __shared__ __attribute__((aligned(16))) u16 sB[64][40];
  int tid=threadIdx.x, lane=tid&63, w=tid>>6;
  int m0=blockIdx.y*64, n0=blockIdx.x*64;
  f32x4 acc[4]={};
  for(int k0=0;k0<K;k0+=32){
    { int r=tid>>2,c=(tid&3)*8; *(u32x4*)&sA[r][c]=*(const u32x4*)&A[(size_t)(m0+r)*lda+k0+c]; }
    { int r=tid>>3,c=(tid&7)*8; u32x4 v=*(const u32x4*)&B[(size_t)(k0+r)*ldb+n0+c]; u16* pv=(u16*)&v;
      for(int j=0;j<8;j++) sB[c+j][r]=pv[j]; }
    __syncthreads();
    bf16x8 a=*(bf16x8*)&sA[w*16+(lane&15)][(lane>>4)*8];
    for(int n=0;n<4;n++){ bf16x8 b=*(bf16x8*)&sB[n*16+(lane&15)][(lane>>4)*8];
      acc[n]=__builtin_amdgcn_mfma_f32_16x16x32_bf16(a,b,acc[n],0,0,0);}
    __syncthreads();
  }
  for(int n=0;n<4;n++)for(int j=0;j<4;j++) C[(size_t)(m0+w*16+(lane>>4)*4+j)*ldc+n0+n*16+(lane&15)]=f2bf(acc[n][j]);
}
__global__ __launch_bounds__(256) void attn(const u16* Q0,const u16* Q1,const u16* K0,const u16* K1,const u16* V0,const u16* V1,u16* O0,u16* O1){
  int br=blockIdx.y; int bh=blockIdx.x; int b=bh>>4,h=bh&15;
  const u16* Q=(br?Q1:Q0)+(size_t)b*48*4096+h*256; const u16* Kp=(br?K1:K0)+(size_t)bh*48*256; const u16* Vp=(br?V1:V0)+(size_t)bh*48*256;
  u16* O=(br?O1:O0)+(size_t)b*48*4096+h*256;
  __shared__ float sS[48][49];
  __shared__ __attribute__((aligned(16))) u16 sV[48][264];
  int tid=threadIdx.x,lane=tid&63,w=tid>>6;
  for(int i=tid;i<48*32;i+=256){int r=i>>5,c=(i&31)*8; *(u32x4*)&sV[r][c]=*(const u32x4*)&Vp[r*256+c];}
  if(w<3){
    f32x4 acc[3]={};
    for(int k=0;k<8;k++){
      bf16x8 a=*(const bf16x8*)&Q[(size_t)(w*16+(lane&15))*4096+k*32+(lane>>4)*8];
      for(int n=0;n<3;n++){ bf16x8 bb=*(const bf16x8*)&Kp[(n*16+(lane&15))*256+k*32+(lane>>4)*8];
        acc[n]=__builtin_amdgcn_mfma_f32_16x16x32_bf16(a,bb,acc[n],0,0,0);}
    }
    for(int n=0;n<3;n++)for(int j=0;j<4;j++) sS[w*16+(lane>>4)*4+j][n*16+(lane&15)]=acc[n][j]*0.0625f;
  }
  __syncthreads();
  if(tid<48){ float m=-1e30f; for(int j=0;j<48;j++) m=fmaxf(m,sS[tid][j]); float s=0; for(int j=0;j<48;j++){float e=__expf(sS[tid][j]-m); sS[tid][j]=e; s+=e;}
    float r=1.f/s; for(int j=0;j<48;j++) sS[tid][j]=bf2f(f2bf(sS[tid][j]*r)); }
  __syncthreads();
  { int d=tid;
    for(int q=0;q<48;q+=4){ float a0=0,a1=0,a2=0,a3=0;
      for(int j=0;j<48;j++){ float v=bf2f(sV[j][d]); a0+=sS[q][j]*v; a1+=sS[q+1][j]*v; a2+=sS[q+2][j]*v; a3+=sS[q+3][j]*v;}
      O[(size_t)q*4096+d]=f2bf(a0); O[(size_t)(q+1)*4096+d]=f2bf(a1); O[(size_t)(q+2)*4096+d]=f2bf(a2); O[(size_t)(q+3)*4096+d]=f2bf(a3);} }
}

struct P { const char* p[10]; int n[10]; };
typedef const __attribute__((address_space(1))) void* gp; typedef __attribute__((address_space(3))) void* lp;
__global__ __launch_bounds__(256) void k_stream(P a, unsigned* out){
  __shared__ __attribute__((aligned(16))) char sm[65536];
  int tid=threadIdx.x; int wid=__builtin_amdgcn_readfirstlane(tid>>6); int l=tid&63;
  const char* cp = a.p[IDX] + (size_t)blockIdx.x*(STRIDE) + wid*1024 + l*16;
  __builtin_amdgcn_global_load_lds((gp)(cp+196*1024),(lp)(sm+wid*1024),16,0,2);
  asm volatile("s_waitcnt vmcnt(0)":::"memory");
  __syncthreads();
  unsigned long long t0=__builtin_amdgcn_s_memrealtime();
  for(int u=0;u<48;u++) __builtin_amdgcn_global_load_lds((gp)(cp+(size_t)u*4096),(lp)(sm+(wid*16+(u&15))*1024),16,0,2);
  asm volatile("s_waitcnt vmcnt(0)":::"memory");
  __syncthreads();
  unsigned long long t1=__builtin_amdgcn_s_memrealtime();
  out[blockIdx.x*256+tid]=*(unsigned*)(sm+tid*4);
  unsigned long long d=t1-t0; if(d>5000) d=5000;
  if(AMP>0){ while(__builtin_amdgcn_s_memrealtime()-t1 < d*AMP){ __builtin_amdgcn_s_sleep(2);} }
}
extern "C" void kernel_launch(void* const* d_in, const int* in_sizes, int n_in, void* d_out, int out_size, void* d_ws, size_t ws_size, hipStream_t stream) {
  const u16* const* in=(const u16* const*)d_in;
  u16* ws=(u16*)d_ws; size_t QS=(size_t)1536*4096;
  u16* Q0=ws,*Q1=ws+QS,*O0=ws+2*QS,*O1=ws+3*QS;
  P a; for(int i=0;i<10;i++){a.p[i]=(const char*)d_in[i]; a.n[i]=in_sizes[i];}
  for(int r=0;r<STREAM;r++) k_stream<<<16,256,0,stream>>>(a,(unsigned*)(ws+4*QS));
  gemm<<<dim3(64,24,2),256,0,stream>>>(in[0],in[5],in[3],in[8],Q0,Q1,448,448,4096,4096);
  attn<<<dim3(512,2),256,0,stream>>>(Q0,Q1,in[1],in[6],in[2],in[7],O0,O1);
  gemm<<<dim3(7,24,2),256,0,stream>>>(O0,O1,in[4],in[9],(u16*)d_out,(u16*)d_out+448,4096,4096,448,896);
}
```

```cpp
#include <hip/hip_runtime.h>

typedef unsigned u32;
typedef unsigned long long u64;
#define KAS __attribute__((address_space(4)))

#define SCHED 0x54105300u
#define ROLE bp
#define DUES 0x3579u
#define DUEL 0x2u
constexpr u32 F_QRDY = 16, F_SRDY = 48;
constexpr u32 WRING = 512, WQ_ROW = 1056, WQ_SLOT = 34304;
constexpr u32 XRING = WRING + 4 * WQ_SLOT, X_SLOT = 12288;
constexpr u32 IMG = 512, IMG_U = 24576;
constexpr u32 LDS_BYTES = 163840;
constexpr u32 O_PIECE = 1056, O_SLOT = 16896, ORING = LDS_BYTES - 8 * O_SLOT;
constexpr u32 SUMS = 512, SUMD = 28672;
static_assert(XRING + 2 * X_SLOT <= LDS_BYTES && SUMS + 24576 <= ORING, "lds");
static_assert(IMG + 4 * IMG_U <= WRING + 3 * WQ_SLOT && ORING + 5 * O_SLOT >= IMG + 4 * IMG_U, "lds");
constexpr u32 WS_EPOCH = 0;
constexpr u32 WS_PART = 4096;
constexpr u32 WSTR = 3200, SSTR = 4 * WSTR, GSTR = 8 * SSTR;
constexpr u32 NOWHERE = 32u * 7u * GSTR;

struct Args {
  const char* x[2];
  const char* k[2];
  const char* v[2];
  const char* wq[2];
  const char* wo[2];
  char* out;
  char* ws;
};

__device__ __forceinline__ int mod7(int x) { return x >= 7 ? x - 7 : x; }

__global__ __launch_bounds__(512, 2) void fused_attn_kernel(Args) {
  const int tid = threadIdx.x;
  int bid = blockIdx.x;
  const KAS Args* kap = (const KAS Args*)__builtin_amdgcn_kernarg_segment_ptr();
  asm volatile(
      "s_and_b32 s78, %[bid], 4\n\t"
      "s_lshl_b32 s78, s78, 1\n\t"
      "s_load_dwordx2 s[74:75], %[ka], s78\n\t"
      "s_add_u32 s78, s78, 48\n\t"
      "s_load_dwordx2 s[76:77], %[ka], s78\n\t"
      "v_readfirstlane_b32 s70, %[tid]\n\t"
      "s_getpc_b64 s[80:81]\n\t"
      "98:\n\t"
      "s_lshr_b32 s70, s70, 6\n\t"
      "s_andn2_b32 s78, %[bid], 7\n\t"
      "s_add_u32 s78, s78, s70\n\t"
      "s_lshl_b32 s78, s78, 7\n\t"
      "s_mov_b32 s79, fa_endc-98b\n\t"
      "s_max_u32 s79, s79, fa_endl-98b\n\t"
      "s_min_u32 s78, s78, s79\n\t"
      "s_add_u32 s80, s80, s78\n\t"
      "s_addc_u32 s81, s81, 0\n\t"
      "s_andn2_b32 s80, s80, 127\n\t"
      "s_lshr_b32 s71, %[bid], 4\n\t"
      "s_sub_u32 s72, s71, 7\n\t"
      "s_cmp_lt_u32 s72, 7\n\t"
      "s_cselect_b32 s72, s72, s71\n\t"
      "s_cselect_b32 s73, 0x40000, 0\n\t"
      "s_mul_i32 s78, s71, 3\n\t"
      "s_sub_u32 s78, s78, 39\n\t"
      "s_cmp_ge_u32 s71, 14\n\t"
      "s_cselect_b32 s72, s78, s72\n\t"
      "v_and_b32 v250, 63, %[tid]\n\t"
      "s_and_b32 s82, s70, 3\n\t"
      "s_mul_i32 s83, s71, 96\n\t"
      "s_cmp_lt_u32 s70, 4\n\t"
      "s_cbranch_scc1 1f\n\t"
      "s_and_b32 s78, %[bid], 3\n\t"
      "s_lshl_b32 s78, s78, 11\n\t"
      "s_and_b32 s79, %[bid], 8\n\t"
      "s_lshl_b32 s79, s79, 7\n\t"
      "s_add_u32 s78, s78, s79\n\t"
      "s_lshl_b32 s79, s72, 19\n\t"
      "s_add_u32 s78, s78, s79\n\t"
      "s_add_u32 s78, s78, s73\n\t"
      "s_lshl_b32 s79, s82, 16\n\t"
      "s_add_u32 s78, s78, s79\n\t"
      "v_lshl_add_u32 v251, v250, 4, s78\n\t"
      "s_mul_i32 s79, s82, 8576\n\t"
      "s_add_u32 m0, s79, 69120\n\t"
      "s_mov_b32 s79, 8\n\t"
      "s_waitcnt lgkmcnt(0)\n\t"
      "s_load_dwordx2 s[84:85], s[80:81], 60\n\t"
      "2:\n\t"
      "global_load_lds_dwordx4 v251, s[76:77]\n\t"
      "v_add_u32 v251, 8192, v251\n\t"
      "s_add_u32 m0, m0, 1056\n\t"
      "s_sub_u32 s79, s79, 1\n\t"
      "s_cmp_lg_u32 s79, 0\n\t"
      "s_cbranch_scc1 2b\n\t"
      "v_lshrrev_b32 v251, 3, v250\n\t"
      "s_mul_i32 s78, s82, 24\n\t"
      "s_add_u32 s78, s78, s83\n\t"
      "v_add_u32 v251, s78, v251\n\t"
      "v_mul_u32_u24 v251, 896, v251\n\t"
      "v_lshrrev_b32 v252, 4, v250\n\t"
      "v_xor_b32 v252, v250, v252\n\t"
      "s_lshl_b32 s78, s82, 2\n\t"
      "v_xor_b32 v252, s78, v252\n\t"
      "v_and_b32 v252, 7, v252\n\t"
      "v_lshl_add_u32 v251, v252, 4, v251\n\t"
      "s_lshl_b32 s78, s72, 7\n\t"
      "v_add_u32 v251, s78, v251\n\t"
      "s_mul_i32 s79, s82, 3072\n\t"
      "s_add_u32 m0, s79, 137728\n\t"
      "s_nop 0\n\t"
      "global_load_lds_dwordx4 v251, s[74:75]\n\t"
      "v_xor_b32 v251, 64, v251\n\t"
      "s_add_u32 m0, m0, 1024\n\t"
      "v_add_u32 v251, 7168, v251\n\t"
      "global_load_lds_dwordx4 v251, s[74:75]\n\t"
      "v_xor_b32 v251, 64, v251\n\t"
      "s_add_u32 m0, m0, 1024\n\t"
      "v_add_u32 v251, 7168, v251\n\t"
      "global_load_lds_dwordx4 v251, s[74:75]\n\t"
      "s_branch 3f\n\t"
      "1:\n\t"
      "v_min_u32 v250, 23, v250\n\t"
      "v_mul_u32_u24 v252, 43, v250\n\t"
      "v_lshrrev_b32 v252, 9, v252\n\t"
      "v_mul_u32_u24 v251, 12, v252\n\t"
      "v_sub_u32 v250, v250, v251\n\t"
      "s_and_b32 s78, %[bid], 8\n\t"
      "s_mul_i32 s78, s78, 6\n\t"
      "s_mul_i32 s79, s82, 12\n\t"
      "s_add_u32 s78, s78, s79\n\t"
      "s_add_u32 s78, s78, s83\n\t"
      "v_add_u32 v250, s78, v250\n\t"
      "v_mul_u32_u24 v250, 896, v250\n\t"
      "s_add_u32 s72, s72, 1\n\t"
      "v_add_u32 v252, s72, v252\n\t"
      "v_subrev_u32 v251, 7, v252\n\t"
      "v_min_u32 v252, v252, v251\n\t"
      "v_lshl_add_u32 v250, v252, 7, v250\n\t"
      "s_movk_i32 m0, 256\n\t"
      "s_waitcnt lgkmcnt(0)\n\t"
      "s_load_dwordx2 s[84:85], s[80:81], 60\n\t"
      "global_load_lds_dword v250, s[74:75]\n\t"
      "3:\n\t"
      : [bid] "+s"(bid), [ka] "+s"(kap)
      : [tid] "v"(tid)
      : "v250", "v251", "v252", "s70", "s71", "s72", "s73", "s74", "s75", "s76", "s77", "s78", "s79", "s80", "s81", "s82", "s83", "s84", "s85", "scc", "memory");
  const KAS Args& a = *kap;
  const int wave = __builtin_amdgcn_readfirstlane(tid >> 6);
  const int xcd = bid & 7, j = bid >> 3;
  const int br = xcd >> 2, hp = (xcd & 3) * 2 + (j & 1), bp = j >> 1;
  const int m = (hp + ROLE) & 7;
  const int grp = br * 16 + bp;
  char* ws = a.ws;
  const int sw = (bp >= 7 && bp < 14) ? 1 : 0, rot = bp >= 14 ? 3 * (bp - 13) : bp - 7 * sw;
  const int g0 = (m < 6) ? m + 1 : 0;
  const char* wog = a.wo[br] + (size_t)hp * 512 * 896;

  if (wave >= 4) {
    const int u = wave - 4;
    const char* xg = a.x[br] + (size_t)bp * 96 * 896;
    const size_t kvoff = ((size_t)(2 * bp + (u & 1)) * 16 + 2 * hp + (u >> 1)) * 24576;
    const u32 l_xsw = (u & 1) << 6, l_xd = XRING + 3 * u * 1024, l_xdx = l_xd ^ (l_xd + X_SLOT), l_wd = WRING + 8 * u * WQ_ROW + 128 * u;
    const char* l_wq = a.wq[br] + hp * 1024 + (size_t)(8 * u) * 8192;
    const char* l_xp = xg + (size_t)(24 * u) * 896;
    const u32 l_rot = rot, l_tsw = sw << 18;
    const char* l_vp = a.v[br] + kvoff;
    const char* l_kp = a.k[br] + kvoff;
    const char* l_wot = wog + (bp * 32 + u * 8) * 896;
    const char* l_xt = xg + ((j & 1) * 48 + u * 12) * 896;
    const int l_pa = (int)((SCHED >> (4 * (bp & 7))) & 15) - 1;
    const u32 l_img = IMG + u * IMG_U, l_qrdy = F_QRDY + 8 * (u >> 1);
    const char* r_part = ws + WS_PART;
    const u32 ob = (u32)(grp * 7 + m) * GSTR + (u & 1) * 2 * WSTR;
    const u32 r_ld = 1 - (u >> 1);
    const u32 r_j0 = m == 7 ? NOWHERE : ob + mod7(m + (r_ld ? 2 : 6)) * SSTR, r_j1 = r_ld ? NOWHERE : ob + mod7(m + 5) * SSTR;
    const u32 r_j2 = ob + mod7(m + 4) * SSTR, r_j3 = ob + mod7(m + 3) * SSTR, r_j4 = NOWHERE, r_due = r_ld ? DUEL : DUES;
    const u32 r_sums = SUMS + r_ld * SUMD + (u & 1) * 12288, r_srdy = F_SRDY + (u & 1) * 16 + r_ld * 4;
    const u32 r_g0 = g0, r_od = ORING + 7 * O_SLOT + (u & 1) * 8 * O_PIECE;
    const char* r_wo = wog + (size_t)((u & 1) * 64) * 896;
    asm volatile(
      "v_mbcnt_lo_u32_b32 v255, -1, 0\n\t"
      "v_mbcnt_hi_u32_b32 v255, -1, v255\n\t"
      "v_lshlrev_b32 v240, 4, v255\n\t"
      "v_add_u32 v241, 8192, v240\n\t"
      "v_add_u32 v242, 16384, v240\n\t"
      "v_add_u32 v243, 24576, v240\n\t"
      "v_add_u32 v244, 32768, v240\n\t"
      "v_add_u32 v245, 40960, v240\n\t"
      "v_add_u32 v246, 49152, v240\n\t"
      "v_add_u32 v247, 57344, v240\n\t"
      "v_lshrrev_b32 v252, 3, v255\n\t"
      "v_mul_u32_u24 v252, 896, v252\n\t"
      "v_and_b32 v253, 7, v255\n\t"
      "v_lshrrev_b32 v254, 4, v255\n\t"
      "v_xor_b32 v253, v253, v254\n\t"
      "v_lshl_add_u32 v252, v253, 4, v252\n\t"
      "v_xor_b32 v248, %[xsw], v252\n\t"
      "v_xor_b32 v249, 64, v248\n\t"
      "v_add_u32 v249, 7168, v249\n\t"
      "v_add_u32 v250, 14336, v248\n\t"
      "s_waitcnt lgkmcnt(0)\n\t"
      "s_mov_b32 s74, 68608\n\t"
      "s_mov_b32 s75, %[xd]\n\t"
      "s_mov_b32 s90, %[rot]\n\t"
      "s_mov_b32 s91, %[tsw]\n\t"
      "s_xor_b32 s75, s75, %[xdx]\n\t"
      "s_xor_b32 s91, s91, 0x40000\n\t"
      "s_add_u32 s74, s74, 34304\n\t"
      "s_cmp_eq_u32 s74, 137216\n\t"
      "s_cselect_b32 s74, 0, s74\n\t"
      "v_and_b32 v253, 15, v255\n\t"
      "v_lshlrev_b32 v251, 4, v253\n\t"
      "v_lshl_add_u32 v251, v254, 11, v251\n\t"
      "s_mov_b32 s76, -1\n\t"
      "s_mov_b64 s[80:81], %[vp]\n\t"
      "s_and_b32 s81, s81, 0xffff\n\t"
      "s_mov_b32 s82, 24576\n\t"
      "s_mov_b32 s83, 0x20000\n\t"
      "s_mov_b64 s[84:85], %[wot]\n\t"
      "s_mov_b64 s[84:85], %[xt]\n\t"
      "s_add_u32 s60, %[rot], 4\n\t"
      "s_sub_u32 s61, s60, 7\n\t"
      "s_min_u32 s60, s60, s61\n\t"
      "s_mov_b32 s62, 10752\n\t"
      "s_mov_b32 s63, 3\n\t"
      "s_mov_b32 s61, 0\n\t"
      "s_mov_b32 s64, 11\n\t"
      "s_mov_b32 s68, 1\n\t"
      "10:\n\t"
      "s_mov_b32 s77, 0\n\t"
      "s_cmp_lg_i32 s76, %[pa]\n\t"
      "s_cbranch_scc1 11f\n\t"
      "v_lshrrev_b32 v252, 5, v255\n\t"
      "v_and_b32 v253, 31, v255\n\t"
      "v_add_u32 v254, 0, v252\n\t"
      "v_xor_b32 v254, v253, v254\n\t"
      "v_lshlrev_b32 v254, 4, v254\n\t"
      "v_lshl_add_u32 v254, v252, 9, v254\n\t"
      "v_add_u32 v144, 0, v254\n\t"
      "v_add_u32 v254, 2, v252\n\t"
      "v_xor_b32 v254, v253, v254\n\t"
      "v_lshlrev_b32 v254, 4, v254\n\t"
      "v_lshl_add_u32 v254, v252, 9, v254\n\t"
      "v_add_u32 v145, 1024, v254\n\t"
      "v_add_u32 v254, 4, v252\n\t"
      "v_xor_b32 v254, v253, v254\n\t"
      "v_lshlrev_b32 v254, 4, v254\n\t"
      "v_lshl_add_u32 v254, v252, 9, v254\n\t"
      "v_add_u32 v146, 2048, v254\n\t"
      "v_add_u32 v254, 6, v252\n\t"
      "v_xor_b32 v254, v253, v254\n\t"
      "v_lshlrev_b32 v254, 4, v254\n\t"
      "v_lshl_add_u32 v254, v252, 9, v254\n\t"
      "v_add_u32 v147, 3072, v254\n\t"
      "v_add_u32 v254, 8, v252\n\t"
      "v_xor_b32 v254, v253, v254\n\t"
      "v_lshlrev_b32 v254, 4, v254\n\t"
      "v_lshl_add_u32 v254, v252, 9, v254\n\t"
      "v_add_u32 v148, 4096, v254\n\t"
      "v_add_u32 v254, 10, v252\n\t"
      "v_xor_b32 v254, v253, v254\n\t"
      "v_lshlrev_b32 v254, 4, v254\n\t"
      "v_lshl_add_u32 v254, v252, 9, v254\n\t"
      "v_add_u32 v149, 5120, v254\n\t"
      "v_add_u32 v254, 12, v252\n\t"
      "v_xor_b32 v254, v253, v254\n\t"
      "v_lshlrev_b32 v254, 4, v254\n\t"
      "v_lshl_add_u32 v254, v252, 9, v254\n\t"
      "v_add_u32 v150, 6144, v254\n\t"
      "v_add_u32 v254, 14, v252\n\t"
      "v_xor_b32 v254, v253, v254\n\t"
      "v_lshlrev_b32 v254, 4, v254\n\t"
      "v_lshl_add_u32 v254, v252, 9, v254\n\t"
      "v_add_u32 v151, 7168, v254\n\t"
      "s_add_u32 s87, s74, %[wd]\n\t"
      "s_add_u32 s88, s74, 34304\n\t"
      "s_cmp_eq_u32 s88, 137216\n\t"
      "s_cselect_b32 s88, 0, s88\n\t"
      "s_add_u32 s88, s88, %[wd]\n\t"
      "s_mov_b64 s[78:79], %[kp]\n\t"
      "s_mov_b32 m0, s87\n\t"
      "s_nop 0\n\t"
      "global_load_lds_dwordx4 v144, s[78:79] nt\n\t"
      "s_add_u32 m0, m0, 1024\n\t"
      "s_nop 0\n\t"
      "global_load_lds_dwordx4 v145, s[78:79] nt\n\t"
      "s_add_u32 m0, m0, 1024\n\t"
      "s_nop 0\n\t"
      "global_load_lds_dwordx4 v146, s[78:79] nt\n\t"
      "s_add_u32 m0, m0, 1024\n\t"
      "s_nop 0\n\t"
      "global_load_lds_dwordx4 v147, s[78:79] nt\n\t"
      "s_add_u32 m0, m0, 1024\n\t"
      "s_nop 0\n\t"
      "global_load_lds_dwordx4 v148, s[78:79] nt\n\t"
      "s_add_u32 m0, m0, 1024\n\t"
      "s_nop 0\n\t"
      "global_load_lds_dwordx4 v149, s[78:79] nt\n\t"
      "s_add_u32 m0, m0, 1024\n\t"
      "s_nop 0\n\t"
      "global_load_lds_dwordx4 v150, s[78:79] nt\n\t"
      "s_add_u32 m0, m0, 1024\n\t"
      "s_nop 0\n\t"
      "global_load_lds_dwordx4 v151, s[78:79] nt\n\t"
      "s_add_u32 s78, s78, 8192\n\t"
      "s_addc_u32 s79, s79, 0\n\t"
      "v_and_b32 v253, 15, v255\n\t"
      "v_xor_b32 v252, v252, v253\n\t"
      "v_bfe_u32 v254, v255, 4, 1\n\t"
      "v_lshlrev_b32 v254, 3, v254\n\t"
      "v_lshl_add_u32 v254, v253, 9, v254\n\t"
      "v_xor_b32 v253, 0, v252\n\t"
      "v_lshl_add_u32 v152, v253, 4, v254\n\t"
      "v_xor_b32 v253, 2, v252\n\t"
      "v_lshl_add_u32 v153, v253, 4, v254\n\t"
      "v_xor_b32 v253, 4, v252\n\t"
      "v_lshl_add_u32 v154, v253, 4, v254\n\t"
      "v_xor_b32 v253, 6, v252\n\t"
      "v_lshl_add_u32 v155, v253, 4, v254\n\t"
      "v_xor_b32 v253, 8, v252\n\t"
      "v_lshl_add_u32 v156, v253, 4, v254\n\t"
      "v_xor_b32 v253, 10, v252\n\t"
      "v_lshl_add_u32 v157, v253, 4, v254\n\t"
      "v_xor_b32 v253, 12, v252\n\t"
      "v_lshl_add_u32 v158, v253, 4, v254\n\t"
      "v_xor_b32 v253, 14, v252\n\t"
      "v_lshl_add_u32 v159, v253, 4, v254\n\t"
      "s_mov_b32 s77, 1\n\t"
      "s_waitcnt vmcnt(8)\n\t"
      "s_barrier\n\t"
      "s_mov_b32 m0, s88\n\t"
      "s_nop 0\n\t"
      "global_load_lds_dwordx4 v144, s[78:79] nt\n\t"
      "s_add_u32 m0, m0, 1024\n\t"
      "s_nop 0\n\t"
      "global_load_lds_dwordx4 v145, s[78:79] nt\n\t"
      "s_add_u32 m0, m0, 1024\n\t"
      "s_nop 0\n\t"
      "global_load_lds_dwordx4 v146, s[78:79] nt\n\t"
      "s_add_u32 m0, m0, 1024\n\t"
      "s_nop 0\n\t"
      "global_load_lds_dwordx4 v147, s[78:79] nt\n\t"
      "s_add_u32 m0, m0, 1024\n\t"
      "s_nop 0\n\t"
      "global_load_lds_dwordx4 v148, s[78:79] nt\n\t"
      "s_add_u32 m0, m0, 1024\n\t"
      "s_nop 0\n\t"
      "global_load_lds_dwordx4 v149, s[78:79] nt\n\t"
      "s_add_u32 m0, m0, 1024\n\t"
      "s_nop 0\n\t"
      "global_load_lds_dwordx4 v150, s[78:79] nt\n\t"
      "s_add_u32 m0, m0, 1024\n\t"
      "s_nop 0\n\t"
      "global_load_lds_dwordx4 v151, s[78:79] nt\n\t"
      "s_add_u32 s78, s78, 8192\n\t"
      "s_addc_u32 s79, s79, 0\n\t"
      "s_movk_i32 s86, 0\n\t"
      "buffer_load_dwordx4 v[192:195], v251, s[80:83], s86 offen offset:0 nt\n\t"
      "s_movk_i32 s86, 0\n\t"
      "buffer_load_dwordx4 v[196:199], v251, s[80:83], s86 offen offset:512 nt\n\t"
      "s_movk_i32 s86, 0\n\t"
      "buffer_load_dwordx4 v[200:203], v251, s[80:83], s86 offen offset:1024 nt\n\t"
      "s_movk_i32 s86, 0\n\t"
      "buffer_load_dwordx4 v[204:207], v251, s[80:83], s86 offen offset:1536 nt\n\t"
      "s_waitcnt vmcnt(12)\n\t"
      "v_add_u32 v160, s87, v152\n\t"
      "v_add_u32 v161, s87, v153\n\t"
      "v_add_u32 v162, s87, v154\n\t"
      "v_add_u32 v163, s87, v155\n\t"
      "v_add_u32 v164, s87, v156\n\t"
      "v_add_u32 v165, s87, v157\n\t"
      "v_add_u32 v166, s87, v158\n\t"
      "v_add_u32 v167, s87, v159\n\t"
      "ds_read_b64 v[0:1], v160 offset:0\n\t"
      "ds_read_b64 v[2:3], v161 offset:0\n\t"
      "ds_read_b64 v[4:5], v162 offset:0\n\t"
      "ds_read_b64 v[6:7], v163 offset:0\n\t"
      "ds_read_b64 v[8:9], v164 offset:0\n\t"
      "ds_read_b64 v[10:11], v165 offset:0\n\t"
      "ds_read_b64 v[12:13], v166 offset:0\n\t"
      "ds_read_b64 v[14:15], v167 offset:0\n\t"
      "ds_read_b64 v[16:17], v160 offset:256\n\t"
      "ds_read_b64 v[18:19], v161 offset:256\n\t"
      "ds_read_b64 v[20:21], v162 offset:256\n\t"
      "ds_read_b64 v[22:23], v163 offset:256\n\t"
      "ds_read_b64 v[24:25], v164 offset:256\n\t"
      "ds_read_b64 v[26:27], v165 offset:256\n\t"
      "ds_read_b64 v[28:29], v166 offset:256\n\t"
      "ds_read_b64 v[30:31], v167 offset:256\n\t"
      "s_waitcnt lgkmcnt(0)\n\t"
      "s_mov_b32 m0, s87\n\t"
      "s_nop 0\n\t"
      "global_load_lds_dwordx4 v144, s[78:79] nt\n\t"
      "s_add_u32 m0, m0, 1024\n\t"
      "s_nop 0\n\t"
      "global_load_lds_dwordx4 v145, s[78:79] nt\n\t"
      "s_add_u32 m0, m0, 1024\n\t"
      "s_nop 0\n\t"
      "global_load_lds_dwordx4 v146, s[78:79] nt\n\t"
      "s_add_u32 m0, m0, 1024\n\t"
      "s_nop 0\n\t"
      "global_load_lds_dwordx4 v147, s[78:79] nt\n\t"
      "s_add_u32 m0, m0, 1024\n\t"
      "s_nop 0\n\t"
      "global_load_lds_dwordx4 v148, s[78:79] nt\n\t"
      "s_add_u32 m0, m0, 1024\n\t"
      "s_nop 0\n\t"
      "global_load_lds_dwordx4 v149, s[78:79] nt\n\t"
      "s_add_u32 m0, m0, 1024\n\t"
      "s_nop 0\n\t"
      "global_load_lds_dwordx4 v150, s[78:79] nt\n\t"
      "s_add_u32 m0, m0, 1024\n\t"
      "s_nop 0\n\t"
      "global_load_lds_dwordx4 v151, s[78:79] nt\n\t"
      "s_add_u32 s78, s78, 8192\n\t"
      "s_addc_u32 s79, s79, 0\n\t"
      "s_movk_i32 s86, 8192\n\t"
      "buffer_load_dwordx4 v[208:211], v251, s[80:83], s86 offen offset:0 nt\n\t"
      "s_movk_i32 s86, 8192\n\t"
      "buffer_load_dwordx4 v[212:215], v251, s[80:83], s86 offen offset:512 nt\n\t"
      "s_movk_i32 s86, 8192\n\t"
      "buffer_load_dwordx4 v[216:219], v251, s[80:83], s86 offen offset:1024 nt\n\t"
      "s_movk_i32 s86, 8192\n\t"
      "buffer_load_dwordx4 v[220:223], v251, s[80:83], s86 offen offset:1536 nt\n\t"
      "s_waitcnt vmcnt(16)\n\t"
      "v_add_u32 v160, s88, v152\n\t"
      "v_add_u32 v161, s88, v153\n\t"
      "v_add_u32 v162, s88, v154\n\t"
      "v_add_u32 v163, s88, v155\n\t"
      "v_add_u32 v164, s88, v156\n\t"
      "v_add_u32 v165, s88, v157\n\t"
      "v_add_u32 v166, s88, v158\n\t"
      "v_add_u32 v167, s88, v159\n\t"
      "ds_read_b64 v[32:33], v160 offset:0\n\t"
      "ds_read_b64 v[34:35], v161 offset:0\n\t"
      "ds_read_b64 v[36:37], v162 offset:0\n\t"
      "ds_read_b64 v[38:39], v163 offset:0\n\t"
      "ds_read_b64 v[40:41], v164 offset:0\n\t"
      "ds_read_b64 v[42:43], v165 offset:0\n\t"
      "ds_read_b64 v[44:45], v166 offset:0\n\t"
      "ds_read_b64 v[46:47], v167 offset:0\n\t"
      "ds_read_b64 v[48:49], v160 offset:256\n\t"
      "ds_read_b64 v[50:51], v161 offset:256\n\t"
      "ds_read_b64 v[52:53], v162 offset:256\n\t"
      "ds_read_b64 v[54:55], v163 offset:256\n\t"
      "ds_read_b64 v[56:57], v164 offset:256\n\t"
      "ds_read_b64 v[58:59], v165 offset:256\n\t"
      "ds_read_b64 v[60:61], v166 offset:256\n\t"
      "ds_read_b64 v[62:63], v167 offset:256\n\t"
      "s_waitcnt vmcnt(4)\n\t"
      "v_add_u32 v160, s87, v152\n\t"
      "v_add_u32 v161, s87, v153\n\t"
      "v_add_u32 v162, s87, v154\n\t"
      "v_add_u32 v163, s87, v155\n\t"
      "v_add_u32 v164, s87, v156\n\t"
      "v_add_u32 v165, s87, v157\n\t"
      "v_add_u32 v166, s87, v158\n\t"
      "v_add_u32 v167, s87, v159\n\t"
      "ds_read_b64 v[64:65], v160 offset:0\n\t"
      "ds_read_b64 v[66:67], v161 offset:0\n\t"
      "ds_read_b64 v[68:69], v162 offset:0\n\t"
      "ds_read_b64 v[70:71], v163 offset:0\n\t"
      "ds_read_b64 v[72:73], v164 offset:0\n\t"
      "ds_read_b64 v[74:75], v165 offset:0\n\t"
      "ds_read_b64 v[76:77], v166 offset:0\n\t"
      "ds_read_b64 v[78:79], v167 offset:0\n\t"
      "ds_read_b64 v[80:81], v160 offset:256\n\t"
      "ds_read_b64 v[82:83], v161 offset:256\n\t"
      "ds_read_b64 v[84:85], v162 offset:256\n\t"
      "ds_read_b64 v[86:87], v163 offset:256\n\t"
      "ds_read_b64 v[88:89], v164 offset:256\n\t"
      "ds_read_b64 v[90:91], v165 offset:256\n\t"
      "ds_read_b64 v[92:93], v166 offset:256\n\t"
      "ds_read_b64 v[94:95], v167 offset:256\n\t"
      "s_waitcnt lgkmcnt(0)\n\t"
      "11:\n\t"
      "s_lshl_b32 s78, s90, 19\n\t"
      "s_add_u32 s78, s78, s91\n\t"
      "s_mov_b64 s[70:71], %[wq]\n\t"
      "s_add_u32 s70, s70, s78\n\t"
      "s_addc_u32 s71, s71, 0\n\t"
      "s_add_u32 m0, s74, %[wd]\n\t"
      "s_nop 0\n\t"
      "global_load_lds_dwordx4 v240, s[70:71]\n\t"
      "s_add_u32 m0, m0, 1056\n\t"
      "s_nop 0\n\t"
      "global_load_lds_dwordx4 v241, s[70:71]\n\t"
      "s_add_u32 m0, m0, 1056\n\t"
      "s_nop 0\n\t"
      "global_load_lds_dwordx4 v242, s[70:71]\n\t"
      "s_add_u32 m0, m0, 1056\n\t"
      "s_nop 0\n\t"
      "global_load_lds_dwordx4 v243, s[70:71]\n\t"
      "s_add_u32 m0, m0, 1056\n\t"
      "s_nop 0\n\t"
      "global_load_lds_dwordx4 v244, s[70:71]\n\t"
      "s_add_u32 m0, m0, 1056\n\t"
      "s_nop 0\n\t"
      "global_load_lds_dwordx4 v245, s[70:71]\n\t"
      "s_add_u32 m0, m0, 1056\n\t"
      "s_nop 0\n\t"
      "global_load_lds_dwordx4 v246, s[70:71]\n\t"
      "s_add_u32 m0, m0, 1056\n\t"
      "s_nop 0\n\t"
      "global_load_lds_dwordx4 v247, s[70:71]\n\t"
      "s_add_u32 s90, s90, 1\n\t"
      "s_cmp_eq_u32 s90, 7\n\t"
      "s_cselect_b32 s90, 0, s90\n\t"
      "s_xor_b32 s91, s91, 0x40000\n\t"
      "s_add_u32 s74, s74, 34304\n\t"
      "s_cmp_eq_u32 s74, 137216\n\t"
      "s_cselect_b32 s74, 0, s74\n\t"
      "s_cmp_lt_i32 s76, -1\n\t"
      "s_cbranch_scc1 17f\n\t"
      "s_sub_u32 s64, s64, 1\n\t"
      "s_cbranch_scc0 15f\n\t"
      "s_mov_b32 s64, 0\n\t"
      "s_branch 16f\n\t"
      "15:\n\t"
      "s_lshl_b32 s65, s60, 7\n\t"
      "s_add_u32 s65, s65, s61\n\t"
      "s_add_u32 s66, s84, s65\n\t"
      "s_addc_u32 s67, s85, 0\n\t"
      "s_load_dwordx2 s[96:97], s[66:67], 60\n\t"
      "s_load_dwordx2 s[96:97], s[66:67], 956\n\t"
      "s_load_dwordx2 s[96:97], s[66:67], 1852\n\t"
      "s_load_dwordx2 s[96:97], s[66:67], 2748\n\t"
      "s_add_u32 s61, s61, 3584\n\t"
      "s_cmp_lg_u32 s61, s62\n\t"
      "s_cbranch_scc1 16f\n\t"
      "s_mov_b32 s61, 0\n\t"
      "s_add_u32 s60, s60, 1\n\t"
      "s_cmp_eq_u32 s60, 7\n\t"
      "s_cselect_b32 s60, 0, s60\n\t"
      "s_sub_u32 s63, s63, 1\n\t"
      "s_cmp_lg_u32 s63, 0\n\t"
      "s_cbranch_scc1 16f\n\t"
      "s_mov_b64 s[84:85], %[wot]\n\t"
      "s_mov_b32 s60, %[g0]\n\t"
      "s_mov_b32 s62, 7168\n\t"
      "16:\n\t"
      "17:\n\t"
      "s_cmp_lg_u32 s77, 0\n\t"
      "s_cbranch_scc1 14f\n\t"
      "s_waitcnt vmcnt(8)\n\t"
      "s_barrier\n\t"
      "14:\n\t"
      "s_lshl_b32 s78, s90, 19\n\t"
      "s_add_u32 s78, s78, s91\n\t"
      "s_mov_b64 s[70:71], %[wq]\n\t"
      "s_add_u32 s70, s70, s78\n\t"
      "s_addc_u32 s71, s71, 0\n\t"
      "s_add_u32 m0, s74, %[wd]\n\t"
      "s_nop 0\n\t"
      "global_load_lds_dwordx4 v240, s[70:71]\n\t"
      "s_add_u32 m0, m0, 1056\n\t"
      "s_nop 0\n\t"
      "global_load_lds_dwordx4 v241, s[70:71]\n\t"
      "s_add_u32 m0, m0, 1056\n\t"
      "s_nop 0\n\t"
      "global_load_lds_dwordx4 v242, s[70:71]\n\t"
      "s_add_u32 m0, m0, 1056\n\t"
      "s_nop 0\n\t"
      "global_load_lds_dwordx4 v243, s[70:71]\n\t"
      "s_add_u32 m0, m0, 1056\n\t"
      "s_nop 0\n\t"
      "global_load_lds_dwordx4 v244, s[70:71]\n\t"
      "s_add_u32 m0, m0, 1056\n\t"
      "s_nop 0\n\t"
      "global_load_lds_dwordx4 v245, s[70:71]\n\t"
      "s_add_u32 m0, m0, 1056\n\t"
      "s_nop 0\n\t"
      "global_load_lds_dwordx4 v246, s[70:71]\n\t"
      "s_add_u32 m0, m0, 1056\n\t"
      "s_nop 0\n\t"
      "global_load_lds_dwordx4 v247, s[70:71]\n\t"
      "s_lshl_b32 s78, s90, 7\n\t"
      "s_mov_b64 s[72:73], %[xp]\n\t"
      "s_add_u32 s72, s72, s78\n\t"
      "s_addc_u32 s73, s73, 0\n\t"
      "s_add_u32 m0, s75, 0\n\t"
      "s_nop 0\n\t"
      "global_load_lds_dwordx4 v248, s[72:73]\n\t"
      "s_add_u32 m0, s75, 1024\n\t"
      "s_nop 0\n\t"
      "global_load_lds_dwordx4 v249, s[72:73]\n\t"
      "s_add_u32 m0, s75, 2048\n\t"
      "s_nop 0\n\t"
      "global_load_lds_dwordx4 v250, s[72:73]\n\t"
      "s_xor_b32 s75, s75, %[xdx]\n\t"
      "s_xor_b32 s91, s91, 0x40000\n\t"
      "s_add_u32 s74, s74, 34304\n\t"
      "s_cmp_eq_u32 s74, 137216\n\t"
      "s_cselect_b32 s74, 0, s74\n\t"
      "s_cmp_lt_i32 s76, -1\n\t"
      "s_cbranch_scc1 17f\n\t"
      "s_sub_u32 s64, s64, 1\n\t"
      "s_cbranch_scc0 15f\n\t"
      "s_mov_b32 s64, 0\n\t"
      "s_branch 16f\n\t"
      "15:\n\t"
      "s_lshl_b32 s65, s60, 7\n\t"
      "s_add_u32 s65, s65, s61\n\t"
      "s_add_u32 s66, s84, s65\n\t"
      "s_addc_u32 s67, s85, 0\n\t"
      "s_load_dwordx2 s[96:97], s[66:67], 60\n\t"
      "s_load_dwordx2 s[96:97], s[66:67], 956\n\t"
      "s_load_dwordx2 s[96:97], s[66:67], 1852\n\t"
      "s_load_dwordx2 s[96:97], s[66:67], 2748\n\t"
      "s_add_u32 s61, s61, 3584\n\t"
      "s_cmp_lg_u32 s61, s62\n\t"
      "s_cbranch_scc1 16f\n\t"
      "s_mov_b32 s61, 0\n\t"
      "s_add_u32 s60, s60, 1\n\t"
      "s_cmp_eq_u32 s60, 7\n\t"
      "s_cselect_b32 s60, 0, s60\n\t"
      "s_sub_u32 s63, s63, 1\n\t"
      "s_cmp_lg_u32 s63, 0\n\t"
      "s_cbranch_scc1 16f\n\t"
      "s_mov_b64 s[84:85], %[wot]\n\t"
      "s_mov_b32 s60, %[g0]\n\t"
      "s_mov_b32 s62, 7168\n\t"
      "16:\n\t"
      "17:\n\t"
      "s_waitcnt vmcnt(11)\n\t"
      "s_barrier\n\t"
      "s_add_i32 s76, s76, 1\n\t"
      "s_cmp_lt_i32 s76, 5\n\t"
      "s_cbranch_scc1 10b\n\t"
      "s_lshl_b32 s78, s90, 19\n\t"
      "s_add_u32 s78, s78, s91\n\t"
      "s_mov_b64 s[70:71], %[wq]\n\t"
      "s_add_u32 s70, s70, s78\n\t"
      "s_addc_u32 s71, s71, 0\n\t"
      "s_add_u32 m0, s74, %[wd]\n\t"
      "s_nop 0\n\t"
      "global_load_lds_dwordx4 v240, s[70:71]\n\t"
      "s_add_u32 m0, m0, 1056\n\t"
      "s_nop 0\n\t"
      "global_load_lds_dwordx4 v241, s[70:71]\n\t"
      "s_add_u32 m0, m0, 1056\n\t"
      "s_nop 0\n\t"
      "global_load_lds_dwordx4 v242, s[70:71]\n\t"
      "s_add_u32 m0, m0, 1056\n\t"
      "s_nop 0\n\t"
      "global_load_lds_dwordx4 v243, s[70:71]\n\t"
      "s_add_u32 m0, m0, 1056\n\t"
      "s_nop 0\n\t"
      "global_load_lds_dwordx4 v244, s[70:71]\n\t"
      "s_add_u32 m0, m0, 1056\n\t"
      "s_nop 0\n\t"
      "global_load_lds_dwordx4 v245, s[70:71]\n\t"
      "s_add_u32 m0, m0, 1056\n\t"
      "s_nop 0\n\t"
      "global_load_lds_dwordx4 v246, s[70:71]\n\t"
      "s_add_u32 m0, m0, 1056\n\t"
      "s_nop 0\n\t"
      "global_load_lds_dwordx4 v247, s[70:71]\n\t"
      "s_add_u32 s90, s90, 1\n\t"
      "s_cmp_eq_u32 s90, 7\n\t"
      "s_cselect_b32 s90, 0, s90\n\t"
      "s_xor_b32 s91, s91, 0x40000\n\t"
      "s_add_u32 s74, s74, 34304\n\t"
      "s_cmp_eq_u32 s74, 137216\n\t"
      "s_cselect_b32 s74, 0, s74\n\t"
      "s_sub_u32 s64, s64, 1\n\t"
      "s_cbranch_scc0 15f\n\t"
      "s_mov_b32 s64, 0\n\t"
      "s_branch 16f\n\t"
      "15:\n\t"
      "s_lshl_b32 s65, s60, 7\n\t"
      "s_add_u32 s65, s65, s61\n\t"
      "s_add_u32 s66, s84, s65\n\t"
      "s_addc_u32 s67, s85, 0\n\t"
      "s_load_dwordx2 s[96:97], s[66:67], 60\n\t"
      "s_load_dwordx2 s[96:97], s[66:67], 956\n\t"
      "s_load_dwordx2 s[96:97], s[66:67], 1852\n\t"
      "s_load_dwordx2 s[96:97], s[66:67], 2748\n\t"
      "s_add_u32 s61, s61, 3584\n\t"
      "s_cmp_lg_u32 s61, s62\n\t"
      "s_cbranch_scc1 16f\n\t"
      "s_mov_b32 s61, 0\n\t"
      "s_add_u32 s60, s60, 1\n\t"
      "s_cmp_eq_u32 s60, 7\n\t"
      "s_cselect_b32 s60, 0, s60\n\t"
      "s_sub_u32 s63, s63, 1\n\t"
      "s_cmp_lg_u32 s63, 0\n\t"
      "s_cbranch_scc1 16f\n\t"
      "s_mov_b64 s[84:85], %[wot]\n\t"
      "s_mov_b32 s60, %[g0]\n\t"
      "s_mov_b32 s62, 7168\n\t"
      "16:\n\t"
      "s_movk_i32 s86, 16384\n\t"
      "buffer_load_dwordx4 v[232:235], v251, s[80:83], s86 offen offset:0 nt\n\t"
      "s_movk_i32 s86, 16384\n\t"
      "buffer_load_dwordx4 v[236:239], v251, s[80:83], s86 offen offset:512 nt\n\t"
      "s_movk_i32 s86, 16384\n\t"
      "buffer_load_dwordx4 v[240:243], v251, s[80:83], s86 offen offset:1024 nt\n\t"
      "s_movk_i32 s86, 16384\n\t"
      "buffer_load_dwordx4 v[244:247], v251, s[80:83], s86 offen offset:1536 nt\n\t"
      "s_movk_i32 s86, 0\n\t"
      "buffer_load_dwordx4 v[144:147], v251, s[80:83], s86 offen offset:256 nt\n\t"
      "s_movk_i32 s86, 0\n\t"
      "buffer_load_dwordx4 v[148:151], v251, s[80:83], s86 offen offset:768 nt\n\t"
      "s_movk_i32 s86, 0\n\t"
      "buffer_load_dwordx4 v[152:155], v251, s[80:83], s86 offen offset:1280 nt\n\t"
      "s_waitcnt vmcnt(15)\n\t"
      "s_barrier\n\t"
      "s_mov_b32 s86, 0x05040100\n\t"
      "s_mov_b32 s87, 0x07060302\n\t"
      "s_sub_u32 s64, s64, 1\n\t"
      "s_cbranch_scc0 15f\n\t"
      "s_mov_b32 s64, 0\n\t"
      "s_branch 16f\n\t"
      "15:\n\t"
      "s_lshl_b32 s65, s60, 7\n\t"
      "s_add_u32 s65, s65, s61\n\t"
      "s_add_u32 s66, s84, s65\n\t"
      "s_addc_u32 s67, s85, 0\n\t"
      "s_load_dwordx2 s[96:97], s[66:67], 60\n\t"
      "s_load_dwordx2 s[96:97], s[66:67], 956\n\t"
      "s_load_dwordx2 s[96:97], s[66:67], 1852\n\t"
      "s_load_dwordx2 s[96:97], s[66:67], 2748\n\t"
      "s_add_u32 s61, s61, 3584\n\t"
      "s_cmp_lg_u32 s61, s62\n\t"
      "s_cbranch_scc1 16f\n\t"
      "s_mov_b32 s61, 0\n\t"
      "s_add_u32 s60, s60, 1\n\t"
      "s_cmp_eq_u32 s60, 7\n\t"
      "s_cselect_b32 s60, 0, s60\n\t"
      "s_sub_u32 s63, s63, 1\n\t"
      "s_cmp_lg_u32 s63, 0\n\t"
      "s_cbranch_scc1 16f\n\t"
      "s_mov_b64 s[84:85], %[wot]\n\t"
      "s_mov_b32 s60, %[g0]\n\t"
      "s_mov_b32 s62, 7168\n\t"
      "16:\n\t"
      "v_perm_b32 v96, v196, v192, s86\n\t"
      "v_perm_b32 v97, v204, v200, s86\n\t"
      "v_perm_b32 v98, v212, v208, s86\n\t"
      "v_perm_b32 v99, v220, v216, s86\n\t"
      "v_perm_b32 v100, v196, v192, s87\n\t"
      "v_perm_b32 v101, v204, v200, s87\n\t"
      "v_perm_b32 v102, v212, v208, s87\n\t"
      "v_perm_b32 v103, v220, v216, s87\n\t"
      "v_perm_b32 v104, v197, v193, s86\n\t"
      "v_perm_b32 v105, v205, v201, s86\n\t"
      "v_perm_b32 v106, v213, v209, s86\n\t"
      "v_perm_b32 v107, v221, v217, s86\n\t"
      "v_perm_b32 v108, v197, v193, s87\n\t"
      "v_perm_b32 v109, v205, v201, s87\n\t"
      "v_perm_b32 v110, v213, v209, s87\n\t"
      "v_perm_b32 v111, v221, v217, s87\n\t"
      "v_perm_b32 v112, v198, v194, s86\n\t"
      "v_perm_b32 v113, v206, v202, s86\n\t"
      "v_perm_b32 v114, v214, v210, s86\n\t"
      "v_perm_b32 v115, v222, v218, s86\n\t"
      "v_perm_b32 v116, v198, v194, s87\n\t"
      "v_perm_b32 v117, v206, v202, s87\n\t"
      "v_perm_b32 v118, v214, v210, s87\n\t"
      "v_perm_b32 v119, v222, v218, s87\n\t"
      "v_perm_b32 v120, v199, v195, s86\n\t"
      "v_perm_b32 v121, v207, v203, s86\n\t"
      "v_perm_b32 v122, v215, v211, s86\n\t"
      "v_perm_b32 v123, v223, v219, s86\n\t"
      "v_perm_b32 v124, v199, v195, s87\n\t"
      "v_perm_b32 v125, v207, v203, s87\n\t"
      "v_perm_b32 v126, v215, v211, s87\n\t"
      "v_perm_b32 v127, v223, v219, s87\n\t"
      "s_waitcnt vmcnt(7)\n\t"
      "s_barrier\n\t"
      "s_barrier\n\t"
      "s_movk_i32 s88, 0\n\t"
      "buffer_load_dwordx4 v[156:159], v251, s[80:83], s88 offen offset:1792 nt\n\t"
      "s_movk_i32 s88, 8192\n\t"
      "buffer_load_dwordx4 v[160:163], v251, s[80:83], s88 offen offset:256 nt\n\t"
      "s_movk_i32 s88, 8192\n\t"
      "buffer_load_dwordx4 v[164:167], v251, s[80:83], s88 offen offset:768 nt\n\t"
      "s_movk_i32 s88, 8192\n\t"
      "buffer_load_dwordx4 v[168:171], v251, s[80:83], s88 offen offset:1280 nt\n\t"
      "s_movk_i32 s88, 8192\n\t"
      "buffer_load_dwordx4 v[172:175], v251, s[80:83], s88 offen offset:1792 nt\n\t"
      "s_movk_i32 s88, 16384\n\t"
      "buffer_load_dwordx4 v[176:179], v251, s[80:83], s88 offen offset:256 nt\n\t"
      "s_movk_i32 s88, 16384\n\t"
      "buffer_load_dwordx4 v[180:183], v251, s[80:83], s88 offen offset:768 nt\n\t"
      "s_movk_i32 s88, 16384\n\t"
      "buffer_load_dwordx4 v[184:187], v251, s[80:83], s88 offen offset:1280 nt\n\t"
      "s_movk_i32 s88, 16384\n\t"
      "buffer_load_dwordx4 v[188:191], v251, s[80:83], s88 offen offset:1792 nt\n\t"
      "v_lshlrev_b32 v252, 4, v255\n\t"
      "v_add_u32 v252, %[img], v252\n\t"
      "v_mov_b32 v253, %[qrdy]\n\t"
      "s_mov_b32 s88, 0x3db8aa3b\n\t"
      "s_waitcnt lgkmcnt(0)\n\t"
      "s_setprio 3\n\t"
      "s_barrier\n\t"
      "ds_read_b128 v[192:195], v252 offset:0\n\t"
      "ds_read_b128 v[196:199], v252 offset:1024\n\t"
      "ds_read_b128 v[200:203], v252 offset:2048\n\t"
      "ds_read_b128 v[204:207], v252 offset:3072\n\t"
      "s_waitcnt lgkmcnt(3)\n\t"
      "v_mfma_f32_16x16x32_bf16 v[216:219], v[0:3], v[192:195], 0\n\t"
      "v_mfma_f32_16x16x32_bf16 v[220:223], v[32:35], v[192:195], 0\n\t"
      "ds_read_b128 v[208:211], v252 offset:4096\n\t"
      "v_mfma_f32_16x16x32_bf16 v[224:227], v[64:67], v[192:195], 0\n\t"
      "s_waitcnt lgkmcnt(3)\n\t"
      "v_mfma_f32_16x16x32_bf16 v[216:219], v[4:7], v[196:199], v[216:219]\n\t"
      "v_mfma_f32_16x16x32_bf16 v[220:223], v[36:39], v[196:199], v[220:223]\n\t"
      "ds_read_b128 v[192:195], v252 offset:5120\n\t"
      "v_mfma_f32_16x16x32_bf16 v[224:227], v[68:71], v[196:199], v[224:227]\n\t"
      "s_waitcnt lgkmcnt(3)\n\t"
      "v_mfma_f32_16x16x32_bf16 v[216:219], v[8:11], v[200:203], v[216:219]\n\t"
      "ds_read_b64 v[254:255], v253\n\t"
      "v_mfma_f32_16x16x32_bf16 v[220:223], v[40:43], v[200:203], v[220:223]\n\t"
      "ds_read_b128 v[196:199], v252 offset:6144\n\t"
      "v_mfma_f32_16x16x32_bf16 v[224:227], v[72:75], v[200:203], v[224:227]\n\t"
      "s_waitcnt vmcnt(12)\n\t"
      "s_waitcnt lgkmcnt(4)\n\t"
      "v_mfma_f32_16x16x32_bf16 v[216:219], v[12:15], v[204:207], v[216:219]\n\t"
      "v_perm_b32 v128, v236, v232, s86\n\t"
      "v_perm_b32 v129, v244, v240, s86\n\t"
      "v_mfma_f32_16x16x32_bf16 v[220:223], v[44:47], v[204:207], v[220:223]\n\t"
      "ds_read_b128 v[200:203], v252 offset:7168\n\t"
      "v_perm_b32 v130, v236, v232, s87\n\t"
      "v_mfma_f32_16x16x32_bf16 v[224:227], v[76:79], v[204:207], v[224:227]\n\t"
      "v_perm_b32 v131, v244, v240, s87\n\t"
      "s_waitcnt lgkmcnt(4)\n\t"
      "v_mfma_f32_16x16x32_bf16 v[216:219], v[16:19], v[208:211], v[216:219]\n\t"
      "s_waitcnt lgkmcnt(2)\n\t"
      "v_min_u32 v254, v254, v255\n\t"
      "v_cmp_le_u32 vcc, 2, v254\n\t"
      "s_cbranch_vccnz 6f\n\t"
      "s_mov_b32 s89, 0x1000000\n\t"
      "1:\n\t"
      "ds_read_b64 v[254:255], v253\n\t"
      "s_waitcnt lgkmcnt(0)\n\t"
      "v_min_u32 v254, v254, v255\n\t"
      "v_cmp_le_u32 vcc, 2, v254\n\t"
      "s_cbranch_vccnz 2f\n\t"
      "s_sleep 1\n\t"
      "s_sub_u32 s89, s89, 1\n\t"
      "s_cmp_lg_u32 s89, 0\n\t"
      "s_cbranch_scc1 1b\n\t"
      "2:\n\t"
      "6:\n\t"
      "v_mfma_f32_16x16x32_bf16 v[220:223], v[48:51], v[208:211], v[220:223]\n\t"
      "ds_read_b128 v[204:207], v252 offset:8192\n\t"
      "v_perm_b32 v132, v237, v233, s86\n\t"
      "v_mfma_f32_16x16x32_bf16 v[224:227], v[80:83], v[208:211], v[224:227]\n\t"
      "v_perm_b32 v133, v245, v241, s86\n\t"
      "v_mfma_f32_16x16x32_bf16 v[216:219], v[20:23], v[192:195], v[216:219]\n\t"
      "v_perm_b32 v134, v237, v233, s87\n\t"
      "v_perm_b32 v135, v245, v241, s87\n\t"
      "v_mfma_f32_16x16x32_bf16 v[220:223], v[52:55], v[192:195], v[220:223]\n\t"
      "ds_read_b128 v[208:211], v252 offset:9216\n\t"
      "v_perm_b32 v136, v238, v234, s86\n\t"
      "v_mfma_f32_16x16x32_bf16 v[224:227], v[84:87], v[192:195], v[224:227]\n\t"
      "v_perm_b32 v137, v246, v242, s86\n\t"
      "s_waitcnt lgkmcnt(3)\n\t"
      "v_mfma_f32_16x16x32_bf16 v[216:219], v[24:27], v[196:199], v[216:219]\n\t"
      "v_perm_b32 v138, v238, v234, s87\n\t"
      "v_perm_b32 v139, v246, v242, s87\n\t"
      "v_mfma_f32_16x16x32_bf16 v[220:223], v[56:59], v[196:199], v[220:223]\n\t"
      "ds_read_b128 v[192:195], v252 offset:10240\n\t"
      "v_perm_b32 v140, v239, v235, s86\n\t"
      "v_mfma_f32_16x16x32_bf16 v[224:227], v[88:91], v[196:199], v[224:227]\n\t"
      "v_perm_b32 v141, v247, v243, s86\n\t"
      "s_waitcnt lgkmcnt(3)\n\t"
      "v_mfma_f32_16x16x32_bf16 v[216:219], v[28:31], v[200:203], v[216:219]\n\t"
      "v_perm_b32 v142, v239, v235, s87\n\t"
      "v_perm_b32 v143, v247, v243, s87\n\t"
      "v_mfma_f32_16x16x32_bf16 v[220:223], v[60:63], v[200:203], v[220:223]\n\t"
      "ds_read_b128 v[196:199], v252 offset:11264\n\t"
      "v_mfma_f32_16x16x32_bf16 v[224:227], v[92:95], v[200:203], v[224:227]\n\t"
      "s_waitcnt lgkmcnt(3)\n\t"
      "v_mfma_f32_16x16x32_bf16 v[228:231], v[0:3], v[204:207], 0\n\t"
      "v_mfma_f32_16x16x32_bf16 v[232:235], v[32:35], v[204:207], 0\n\t"
      "ds_read_b128 v[200:203], v252 offset:12288\n\t"
      "v_mfma_f32_16x16x32_bf16 v[236:239], v[64:67], v[204:207], 0\n\t"
      "v_max3_f32 v212, v216, v217, v218\n\t"
      "v_max3_f32 v213, v219, v220, v221\n\t"
      "v_max3_f32 v212, v212, v222, v223\n\t"
      "v_max3_f32 v213, v213, v224, v225\n\t"
      "s_waitcnt lgkmcnt(3)\n\t"
      "v_mfma_f32_16x16x32_bf16 v[228:231], v[4:7], v[208:211], v[228:231]\n\t"
      "v_max3_f32 v212, v212, v226, v227\n\t"
      "v_max_f32 v212, v212, v213\n\t"
      "v_mov_b32 v213, v212\n\t"
      "v_mfma_f32_16x16x32_bf16 v[232:235], v[36:39], v[208:211], v[232:235]\n\t"
      "ds_read_b128 v[204:207], v252 offset:13312\n\t"
      "v_permlane32_swap_b32 v212, v213\n\t"
      "v_max_f32 v212, v212, v213\n\t"
      "v_mov_b32 v213, v212\n\t"
      "v_mfma_f32_16x16x32_bf16 v[236:239], v[68:71], v[208:211], v[236:239]\n\t"
      "s_waitcnt lgkmcnt(3)\n\t"
      "v_mfma_f32_16x16x32_bf16 v[228:231], v[8:11], v[192:195], v[228:231]\n\t"
      "ds_read_b64 v[254:255], v253\n\t"
      "v_permlane16_swap_b32 v212, v213\n\t"
      "v_max_f32 v212, v212, v213\n\t"
      "v_mul_f32 v212, s88, v212\n\t"
      "v_fma_f32 v216, v216, s88, -v212\n\t"
      "v_mfma_f32_16x16x32_bf16 v[232:235], v[40:43], v[192:195], v[232:235]\n\t"
      "ds_read_b128 v[208:211], v252 offset:14336\n\t"
      "v_fma_f32 v217, v217, s88, -v212\n\t"
      "v_fma_f32 v218, v218, s88, -v212\n\t"
      "v_fma_f32 v219, v219, s88, -v212\n\t"
      "v_fma_f32 v220, v220, s88, -v212\n\t"
      "v_mfma_f32_16x16x32_bf16 v[236:239], v[72:75], v[192:195], v[236:239]\n\t"
      "v_fma_f32 v221, v221, s88, -v212\n\t"
      "v_fma_f32 v222, v222, s88, -v212\n\t"
      "v_fma_f32 v223, v223, s88, -v212\n\t"
      "v_fma_f32 v224, v224, s88, -v212\n\t"
      "s_waitcnt lgkmcnt(4)\n\t"
      "v_mfma_f32_16x16x32_bf16 v[228:231], v[12:15], v[196:199], v[228:231]\n\t"
      "v_fma_f32 v225, v225, s88, -v212\n\t"
      "v_fma_f32 v226, v226, s88, -v212\n\t"
      "v_fma_f32 v227, v227, s88, -v212\n\t"
      "v_exp_f32 v216, v216\n\t"
      "v_mfma_f32_16x16x32_bf16 v[232:235], v[44:47], v[196:199], v[232:235]\n\t"
      "ds_read_b128 v[192:195], v252 offset:15360\n\t"
      "v_exp_f32 v217, v217\n\t"
      "v_exp_f32 v218, v218\n\t"
      "v_mfma_f32_16x16x32_bf16 v[236:239], v[76:79], v[196:199], v[236:239]\n\t"
      "v_add_f32 v214, v216, v217\n\t"
      "v_exp_f32 v219, v219\n\t"
      "v_add_f32 v214, v214, v218\n\t"
      "s_waitcnt lgkmcnt(4)\n\t"
      "v_mfma_f32_16x16x32_bf16 v[228:231], v[16:19], v[200:203], v[228:231]\n\t"
      "s_waitcnt lgkmcnt(2)\n\t"
      "v_min_u32 v254, v254, v255\n\t"
      "v_cmp_le_u32 vcc, 3, v254\n\t"
      "s_cbranch_vccnz 6f\n\t"
      "s_mov_b32 s89, 0x1000000\n\t"
      "1:\n\t"
      "ds_read_b64 v[254:255], v253\n\t"
      "s_waitcnt lgkmcnt(0)\n\t"
      "v_min_u32 v254, v254, v255\n\t"
      "v_cmp_le_u32 vcc, 3, v254\n\t"
      "s_cbranch_vccnz 2f\n\t"
      "s_sleep 1\n\t"
      "s_sub_u32 s89, s89, 1\n\t"
      "s_cmp_lg_u32 s89, 0\n\t"
      "s_cbranch_scc1 1b\n\t"
      "2:\n\t"
      "6:\n\t"
      "v_mfma_f32_16x16x32_bf16 v[232:235], v[48:51], v[200:203], v[232:235]\n\t"
      "ds_read_b128 v[196:199], v252 offset:16384\n\t"
      "v_exp_f32 v220, v220\n\t"
      "v_add_f32 v214, v214, v219\n\t"
      "v_exp_f32 v221, v221\n\t"
      "v_mfma_f32_16x16x32_bf16 v[236:239], v[80:83], v[200:203], v[236:239]\n\t"
      "v_add_f32 v214, v214, v220\n\t"
      "v_exp_f32 v222, v222\n\t"
      "v_add_f32 v214, v214, v221\n\t"
      "v_mfma_f32_16x16x32_bf16 v[228:231], v[20:23], v[204:207], v[228:231]\n\t"
      "v_exp_f32 v223, v223\n\t"
      "v_add_f32 v214, v214, v222\n\t"
      "v_exp_f32 v224, v224\n\t"
      "v_add_f32 v214, v214, v223\n\t"
      "v_mfma_f32_16x16x32_bf16 v[232:235], v[52:55], v[204:207], v[232:235]\n\t"
      "ds_read_b128 v[200:203], v252 offset:17408\n\t"
      "v_exp_f32 v225, v225\n\t"
      "v_add_f32 v214, v214, v224\n\t"
      "v_exp_f32 v226, v226\n\t"
      "v_mfma_f32_16x16x32_bf16 v[236:239], v[84:87], v[204:207], v[236:239]\n\t"
      "v_add_f32 v214, v214, v225\n\t"
      "v_exp_f32 v227, v227\n\t"
      "v_add_f32 v214, v214, v226\n\t"
      "v_add_f32 v214, v214, v227\n\t"
      "s_waitcnt lgkmcnt(3)\n\t"
      "v_mfma_f32_16x16x32_bf16 v[228:231], v[24:27], v[208:211], v[228:231]\n\t"
      "v_mov_b32 v213, v214\n\t"
      "v_mfma_f32_16x16x32_bf16 v[232:235], v[56:59], v[208:211], v[232:235]\n\t"
      "ds_read_b128 v[204:207], v252 offset:18432\n\t"
      "v_permlane32_swap_b32 v214, v213\n\t"
      "v_add_f32 v214, v214, v213\n\t"
      "v_mov_b32 v213, v214\n\t"
      "v_mfma_f32_16x16x32_bf16 v[236:239], v[88:91], v[208:211], v[236:239]\n\t"
      "s_waitcnt lgkmcnt(3)\n\t"
      "v_mfma_f32_16x16x32_bf16 v[228:231], v[28:31], v[192:195], v[228:231]\n\t"
      "v_permlane16_swap_b32 v214, v213\n\t"
      "v_add_f32 v214, v214, v213\n\t"
      "v_rcp_f32 v214, v214\n\t"
      "s_nop 0\n\t"
      "v_mul_f32 v216, v216, v214\n\t"
      "v_mul_f32 v217, v217, v214\n\t"
      "v_cvt_pk_bf16_f32 v216, v216, v217\n\t"
      "v_mul_f32 v218, v218, v214\n\t"
      "v_mfma_f32_16x16x32_bf16 v[232:235], v[60:63], v[192:195], v[232:235]\n\t"
      "ds_read_b128 v[208:211], v252 offset:19456\n\t"
      "v_mul_f32 v219, v219, v214\n\t"
      "v_cvt_pk_bf16_f32 v217, v218, v219\n\t"
      "v_mul_f32 v220, v220, v214\n\t"
      "v_mul_f32 v221, v221, v214\n\t"
      "v_cvt_pk_bf16_f32 v218, v220, v221\n\t"
      "v_mul_f32 v222, v222, v214\n\t"
      "v_mul_f32 v223, v223, v214\n\t"
      "v_mfma_f32_16x16x32_bf16 v[236:239], v[92:95], v[192:195], v[236:239]\n\t"
      "v_cvt_pk_bf16_f32 v219, v222, v223\n\t"
      "v_mul_f32 v224, v224, v214\n\t"
      "v_mul_f32 v225, v225, v214\n\t"
      "v_cvt_pk_bf16_f32 v220, v224, v225\n\t"
      "v_mul_f32 v226, v226, v214\n\t"
      "v_mul_f32 v227, v227, v214\n\t"
      "v_cvt_pk_bf16_f32 v221, v226, v227\n\t"
      "v_mov_b64 v[222:223], 0\n\t"
      "v_mov_b64 v[224:225], v[220:221]\n\t"
      "s_waitcnt lgkmcnt(3)\n\t"
      "v_mfma_f32_16x16x32_bf16 v[240:243], v[0:3], v[196:199], 0\n\t"
      "v_mfma_f32_16x16x32_bf16 v[244:247], v[32:35], v[196:199], 0\n\t"
      "ds_read_b128 v[192:195], v252 offset:20480\n\t"
      "v_mfma_f32_16x16x32_bf16 v[248:251], v[64:67], v[196:199], 0\n\t"
      "v_max3_f32 v212, v228, v229, v230\n\t"
      "v_max3_f32 v213, v231, v232, v233\n\t"
      "v_max3_f32 v212, v212, v234, v235\n\t"
      "v_max3_f32 v213, v213, v236, v237\n\t"
      "s_waitcnt lgkmcnt(3)\n\t"
      "v_mfma_f32_16x16x32_bf16 v[240:243], v[4:7], v[200:203], v[240:243]\n\t"
      "v_max3_f32 v212, v212, v238, v239\n\t"
      "v_max_f32 v212, v212, v213\n\t"
      "v_mov_b32 v213, v212\n\t"
      "v_mfma_f32_16x16x32_bf16 v[244:247], v[36:39], v[200:203], v[244:247]\n\t"
      "ds_read_b128 v[196:199], v252 offset:21504\n\t"
      "v_permlane32_swap_b32 v212, v213\n\t"
      "v_max_f32 v212, v212, v213\n\t"
      "v_mov_b32 v213, v212\n\t"
      "v_mfma_f32_16x16x32_bf16 v[248:251], v[68:71], v[200:203], v[248:251]\n\t"
      "s_waitcnt lgkmcnt(3)\n\t"
      "v_mfma_f32_16x16x32_bf16 v[240:243], v[8:11], v[204:207], v[240:243]\n\t"
      "v_permlane16_swap_b32 v212, v213\n\t"
      "v_max_f32 v212, v212, v213\n\t"
      "v_mul_f32 v212, s88, v212\n\t"
      "v_fma_f32 v228, v228, s88, -v212\n\t"
      "v_fma_f32 v229, v229, s88, -v212\n\t"
      "v_mfma_f32_16x16x32_bf16 v[244:247], v[40:43], v[204:207], v[244:247]\n\t"
      "ds_read_b128 v[200:203], v252 offset:22528\n\t"
      "v_fma_f32 v230, v230, s88, -v212\n\t"
      "v_fma_f32 v231, v231, s88, -v212\n\t"
      "v_fma_f32 v232, v232, s88, -v212\n\t"
      "v_fma_f32 v233, v233, s88, -v212\n\t"
      "v_mfma_f32_16x16x32_bf16 v[248:251], v[72:75], v[204:207], v[248:251]\n\t"
      "v_fma_f32 v234, v234, s88, -v212\n\t"
      "v_fma_f32 v235, v235, s88, -v212\n\t"
      "v_fma_f32 v236, v236, s88, -v212\n\t"
      "v_fma_f32 v237, v237, s88, -v212\n\t"
      "s_waitcnt lgkmcnt(3)\n\t"
      "v_mfma_f32_16x16x32_bf16 v[240:243], v[12:15], v[208:211], v[240:243]\n\t"
      "v_fma_f32 v238, v238, s88, -v212\n\t"
      "v_fma_f32 v239, v239, s88, -v212\n\t"
      "v_exp_f32 v228, v228\n\t"
      "v_mfma_f32_16x16x32_bf16 v[244:247], v[44:47], v[208:211], v[244:247]\n\t"
      "ds_read_b128 v[204:207], v252 offset:23552\n\t"
      "v_exp_f32 v229, v229\n\t"
      "v_exp_f32 v230, v230\n\t"
      "v_mfma_f32_16x16x32_bf16 v[248:251], v[76:79], v[208:211], v[248:251]\n\t"
      "v_add_f32 v214, v228, v229\n\t"
      "v_exp_f32 v231, v231\n\t"
      "v_add_f32 v214, v214, v230\n\t"
      "s_waitcnt lgkmcnt(3)\n\t"
      "v_mfma_f32_16x16x32_bf16 v[240:243], v[16:19], v[192:195], v[240:243]\n\t"
      "v_exp_f32 v232, v232\n\t"
      "v_add_f32 v214, v214, v231\n\t"
      "v_exp_f32 v233, v233\n\t"
      "v_mfma_f32_16x16x32_bf16 v[244:247], v[48:51], v[192:195], v[244:247]\n\t"
      "v_add_f32 v214, v214, v232\n\t"
      "v_exp_f32 v234, v234\n\t"
      "v_add_f32 v214, v214, v233\n\t"
      "v_mfma_f32_16x16x32_bf16 v[248:251], v[80:83], v[192:195], v[248:251]\n\t"
      "v_exp_f32 v235, v235\n\t"
      "v_add_f32 v214, v214, v234\n\t"
      "s_waitcnt lgkmcnt(2)\n\t"
      "v_mfma_f32_16x16x32_bf16 v[240:243], v[20:23], v[196:199], v[240:243]\n\t"
      "v_exp_f32 v236, v236\n\t"
      "v_add_f32 v214, v214, v235\n\t"
      "v_exp_f32 v237, v237\n\t"
      "v_mfma_f32_16x16x32_bf16 v[244:247], v[52:55], v[196:199], v[244:247]\n\t"
      "v_add_f32 v214, v214, v236\n\t"
      "v_exp_f32 v238, v238\n\t"
      "v_add_f32 v214, v214, v237\n\t"
      "v_mfma_f32_16x16x32_bf16 v[248:251], v[84:87], v[196:199], v[248:251]\n\t"
      "v_exp_f32 v239, v239\n\t"
      "v_add_f32 v214, v214, v238\n\t"
      "v_add_f32 v214, v214, v239\n\t"
      "v_mov_b32 v213, v214\n\t"
      "s_waitcnt lgkmcnt(1)\n\t"
      "v_mfma_f32_16x16x32_bf16 v[240:243], v[24:27], v[200:203], v[240:243]\n\t"
      "v_permlane32_swap_b32 v214, v213\n\t"
      "v_add_f32 v214, v214, v213\n\t"
      "v_mov_b32 v213, v214\n\t"
      "v_mfma_f32_16x16x32_bf16 v[244:247], v[56:59], v[200:203], v[244:247]\n\t"
      "v_mfma_f32_16x16x32_bf16 v[248:251], v[88:91], v[200:203], v[248:251]\n\t"
      "v_permlane16_swap_b32 v214, v213\n\t"
      "v_add_f32 v214, v214, v213\n\t"
      "v_rcp_f32 v214, v214\n\t"
      "s_nop 0\n\t"
      "v_mul_f32 v228, v228, v214\n\t"
      "v_mul_f32 v229, v229, v214\n\t"
      "s_waitcnt lgkmcnt(0)\n\t"
      "v_mfma_f32_16x16x32_bf16 v[240:243], v[28:31], v[204:207], v[240:243]\n\t"
      "v_cvt_pk_bf16_f32 v228, v228, v229\n\t"
      "v_mul_f32 v230, v230, v214\n\t"
      "v_mul_f32 v231, v231, v214\n\t"
      "v_cvt_pk_bf16_f32 v229, v230, v231\n\t"
      "v_mul_f32 v232, v232, v214\n\t"
      "v_mul_f32 v233, v233, v214\n\t"
      "v_mfma_f32_16x16x32_bf16 v[244:247], v[60:63], v[204:207], v[244:247]\n\t"
      "v_cvt_pk_bf16_f32 v230, v232, v233\n\t"
      "v_mul_f32 v234, v234, v214\n\t"
      "v_mul_f32 v235, v235, v214\n\t"
      "v_cvt_pk_bf16_f32 v231, v234, v235\n\t"
      "v_mul_f32 v236, v236, v214\n\t"
      "v_mul_f32 v237, v237, v214\n\t"
      "v_mfma_f32_16x16x32_bf16 v[248:251], v[92:95], v[204:207], v[248:251]\n\t"
      "v_cvt_pk_bf16_f32 v232, v236, v237\n\t"
      "v_mul_f32 v238, v238, v214\n\t"
      "v_mul_f32 v239, v239, v214\n\t"
      "v_cvt_pk_bf16_f32 v233, v238, v239\n\t"
      "v_mov_b64 v[234:235], 0\n\t"
      "v_mov_b64 v[236:237], v[232:233]\n\t"
      "v_mfma_f32_16x16x32_bf16 v[48:51], v[96:99], v[216:219], 0\n\t"
      "v_max3_f32 v212, v240, v241, v242\n\t"
      "v_max3_f32 v213, v243, v244, v245\n\t"
      "v_max3_f32 v212, v212, v246, v247\n\t"
      "v_max3_f32 v213, v213, v248, v249\n\t"
      "v_max3_f32 v212, v212, v250, v251\n\t"
      "v_mfma_f32_16x16x32_bf16 v[48:51], v[128:131], v[220:223], v[48:51]\n\t"
      "v_max_f32 v212, v212, v213\n\t"
      "v_mov_b32 v213, v212\n\t"
      "v_mfma_f32_16x16x32_bf16 v[52:55], v[128:131], v[222:225], 0\n\t"
      "v_mfma_f32_16x16x32_bf16 v[52:55], v[100:103], v[216:219], v[52:55]\n\t"
      "v_mfma_f32_16x16x32_bf16 v[56:59], v[104:107], v[216:219], 0\n\t"
      "v_permlane32_swap_b32 v212, v213\n\t"
      "v_max_f32 v212, v212, v213\n\t"
      "v_mov_b32 v213, v212\n\t"
      "v_mfma_f32_16x16x32_bf16 v[56:59], v[132:135], v[220:223], v[56:59]\n\t"
      "v_cvt_pk_bf16_f32 v68, v48, v49\n\t"
      "v_cvt_pk_bf16_f32 v69, v50, v51\n\t"
      "v_permlane16_swap_b32 v212, v213\n\t"
      "v_max_f32 v212, v212, v213\n\t"
      "v_mul_f32 v212, s88, v212\n\t"
      "v_fma_f32 v240, v240, s88, -v212\n\t"
      "v_mfma_f32_16x16x32_bf16 v[60:63], v[132:135], v[222:225], 0\n\t"
      "v_cvt_pk_bf16_f32 v70, v52, v53\n\t"
      "v_cvt_pk_bf16_f32 v71, v54, v55\n\t"
      "v_fma_f32 v241, v241, s88, -v212\n\t"
      "v_fma_f32 v242, v242, s88, -v212\n\t"
      "v_fma_f32 v243, v243, s88, -v212\n\t"
      "v_fma_f32 v244, v244, s88, -v212\n\t"
      "v_mfma_f32_16x16x32_bf16 v[60:63], v[108:111], v[216:219], v[60:63]\n\t"
      "ds_write_b128 v252, v[68:71] offset:0\n\t"
      "v_mfma_f32_16x16x32_bf16 v[48:51], v[112:115], v[216:219], 0\n\t"
      "v_fma_f32 v245, v245, s88, -v212\n\t"
      "v_fma_f32 v246, v246, s88, -v212\n\t"
      "v_fma_f32 v247, v247, s88, -v212\n\t"
      "v_fma_f32 v248, v248, s88, -v212\n\t"
      "v_fma_f32 v249, v249, s88, -v212\n\t"
      "v_fma_f32 v250, v250, s88, -v212\n\t"
      "v_mfma_f32_16x16x32_bf16 v[48:51], v[136:139], v[220:223], v[48:51]\n\t"
      "v_cvt_pk_bf16_f32 v64, v56, v57\n\t"
      "v_cvt_pk_bf16_f32 v65, v58, v59\n\t"
      "v_fma_f32 v251, v251, s88, -v212\n\t"
      "v_exp_f32 v240, v240\n\t"
      "v_mfma_f32_16x16x32_bf16 v[52:55], v[136:139], v[222:225], 0\n\t"
      "v_cvt_pk_bf16_f32 v66, v60, v61\n\t"
      "v_cvt_pk_bf16_f32 v67, v62, v63\n\t"
      "v_exp_f32 v241, v241\n\t"
      "v_exp_f32 v242, v242\n\t"
      "v_mfma_f32_16x16x32_bf16 v[52:55], v[116:119], v[216:219], v[52:55]\n\t"
      "ds_write_b128 v252, v[64:67] offset:1024\n\t"
      "v_mfma_f32_16x16x32_bf16 v[56:59], v[120:123], v[216:219], 0\n\t"
      "v_add_f32 v214, v240, v241\n\t"
      "v_exp_f32 v243, v243\n\t"
      "v_add_f32 v214, v214, v242\n\t"
      "v_exp_f32 v244, v244\n\t"
      "v_mfma_f32_16x16x32_bf16 v[56:59], v[140:143], v[220:223], v[56:59]\n\t"
      "v_cvt_pk_bf16_f32 v68, v48, v49\n\t"
      "v_cvt_pk_bf16_f32 v69, v50, v51\n\t"
      "v_add_f32 v214, v214, v243\n\t"
      "v_exp_f32 v245, v245\n\t"
      "v_add_f32 v214, v214, v244\n\t"
      "v_mfma_f32_16x16x32_bf16 v[60:63], v[140:143], v[222:225], 0\n\t"
      "v_cvt_pk_bf16_f32 v70, v52, v53\n\t"
      "v_cvt_pk_bf16_f32 v71, v54, v55\n\t"
      "v_exp_f32 v246, v246\n\t"
      "v_add_f32 v214, v214, v245\n\t"
      "v_mfma_f32_16x16x32_bf16 v[60:63], v[124:127], v[216:219], v[60:63]\n\t"
      "ds_write_b128 v252, v[68:71] offset:2048\n\t"
      "v_mfma_f32_16x16x32_bf16 v[48:51], v[96:99], v[228:231], 0\n\t"
      "v_exp_f32 v247, v247\n\t"
      "v_add_f32 v214, v214, v246\n\t"
      "v_exp_f32 v248, v248\n\t"
      "v_mfma_f32_16x16x32_bf16 v[48:51], v[128:131], v[232:235], v[48:51]\n\t"
      "v_cvt_pk_bf16_f32 v64, v56, v57\n\t"
      "v_cvt_pk_bf16_f32 v65, v58, v59\n\t"
      "v_add_f32 v214, v214, v247\n\t"
      "v_exp_f32 v249, v249\n\t"
      "v_mfma_f32_16x16x32_bf16 v[52:55], v[128:131], v[234:237], 0\n\t"
      "v_cvt_pk_bf16_f32 v66, v60, v61\n\t"
      "v_cvt_pk_bf16_f32 v67, v62, v63\n\t"
      "v_add_f32 v214, v214, v248\n\t"
      "v_exp_f32 v250, v250\n\t"
      "v_mfma_f32_16x16x32_bf16 v[52:55], v[100:103], v[228:231], v[52:55]\n\t"
      "ds_write_b128 v252, v[64:67] offset:3072\n\t"
      "v_mfma_f32_16x16x32_bf16 v[56:59], v[104:107], v[228:231], 0\n\t"
      "v_add_f32 v214, v214, v249\n\t"
      "v_exp_f32 v251, v251\n\t"
      "v_add_f32 v214, v214, v250\n\t"
      "v_add_f32 v214, v214, v251\n\t"
      "v_mfma_f32_16x16x32_bf16 v[56:59], v[132:135], v[232:235], v[56:59]\n\t"
      "v_cvt_pk_bf16_f32 v68, v48, v49\n\t"
      "v_cvt_pk_bf16_f32 v69, v50, v51\n\t"
      "v_mov_b32 v213, v214\n\t"
      "v_mfma_f32_16x16x32_bf16 v[60:63], v[132:135], v[234:237], 0\n\t"
      "v_cvt_pk_bf16_f32 v70, v52, v53\n\t"
      "v_cvt_pk_bf16_f32 v71, v54, v55\n\t"
      "v_permlane32_swap_b32 v214, v213\n\t"
      "v_add_f32 v214, v214, v213\n\t"
      "v_mov_b32 v213, v214\n\t"
      "v_mfma_f32_16x16x32_bf16 v[60:63], v[108:111], v[228:231], v[60:63]\n\t"
      "ds_write_b128 v252, v[68:71] offset:8192\n\t"
      "v_mfma_f32_16x16x32_bf16 v[48:51], v[112:115], v[228:231], 0\n\t"
      "v_permlane16_swap_b32 v214, v213\n\t"
      "v_add_f32 v214, v214, v213\n\t"
      "v_rcp_f32 v214, v214\n\t"
      "s_nop 0\n\t"
      "v_mul_f32 v240, v240, v214\n\t"
      "v_mul_f32 v241, v241, v214\n\t"
      "v_mfma_f32_16x16x32_bf16 v[48:51], v[136:139], v[232:235], v[48:51]\n\t"
      "v_cvt_pk_bf16_f32 v64, v56, v57\n\t"
      "v_cvt_pk_bf16_f32 v65, v58, v59\n\t"
      "v_cvt_pk_bf16_f32 v240, v240, v241\n\t"
      "v_mul_f32 v242, v242, v214\n\t"
      "v_mul_f32 v243, v243, v214\n\t"
      "v_cvt_pk_bf16_f32 v241, v242, v243\n\t"
      "v_mfma_f32_16x16x32_bf16 v[52:55], v[136:139], v[234:237], 0\n\t"
      "v_cvt_pk_bf16_f32 v66, v60, v61\n\t"
      "v_cvt_pk_bf16_f32 v67, v62, v63\n\t"
      "v_mul_f32 v244, v244, v214\n\t"
      "v_mul_f32 v245, v245, v214\n\t"
      "v_cvt_pk_bf16_f32 v242, v244, v245\n\t"
      "v_mul_f32 v246, v246, v214\n\t"
      "v_mfma_f32_16x16x32_bf16 v[52:55], v[116:119], v[228:231], v[52:55]\n\t"
      "ds_write_b128 v252, v[64:67] offset:9216\n\t"
      "v_mfma_f32_16x16x32_bf16 v[56:59], v[120:123], v[228:231], 0\n\t"
      "v_mul_f32 v247, v247, v214\n\t"
      "v_cvt_pk_bf16_f32 v243, v246, v247\n\t"
      "v_mul_f32 v248, v248, v214\n\t"
      "v_mul_f32 v249, v249, v214\n\t"
      "v_cvt_pk_bf16_f32 v244, v248, v249\n\t"
      "v_mfma_f32_16x16x32_bf16 v[56:59], v[140:143], v[232:235], v[56:59]\n\t"
      "v_cvt_pk_bf16_f32 v68, v48, v49\n\t"
      "v_cvt_pk_bf16_f32 v69, v50, v51\n\t"
      "v_mul_f32 v250, v250, v214\n\t"
      "v_mul_f32 v251, v251, v214\n\t"
      "v_cvt_pk_bf16_f32 v245, v250, v251\n\t"
      "v_mfma_f32_16x16x32_bf16 v[60:63], v[140:143], v[234:237], 0\n\t"
      "v_cvt_pk_bf16_f32 v70, v52, v53\n\t"
      "v_cvt_pk_bf16_f32 v71, v54, v55\n\t"
      "v_mov_b64 v[246:247], 0\n\t"
      "v_mov_b64 v[248:249], v[244:245]\n\t"
      "v_mfma_f32_16x16x32_bf16 v[60:63], v[124:127], v[228:231], v[60:63]\n\t"
      "ds_write_b128 v252, v[68:71] offset:10240\n\t"
      "s_waitcnt vmcnt(0)\n\t"
      "v_mfma_f32_16x16x32_bf16 v[48:51], v[96:99], v[240:243], 0\n\t"
      "v_perm_b32 v0, v148, v144, s86\n\t"
      "v_perm_b32 v1, v156, v152, s86\n\t"
      "v_perm_b32 v2, v164, v160, s86\n\t"
      "v_perm_b32 v3, v172, v168, s86\n\t"
      "v_mfma_f32_16x16x32_bf16 v[48:51], v[128:131], v[244:247], v[48:51]\n\t"
      "v_cvt_pk_bf16_f32 v64, v56, v57\n\t"
      "v_cvt_pk_bf16_f32 v65, v58, v59\n\t"
      "v_perm_b32 v32, v180, v176, s86\n\t"
      "v_perm_b32 v33, v188, v184, s86\n\t"
      "v_mfma_f32_16x16x32_bf16 v[52:55], v[128:131], v[246:249], 0\n\t"
      "v_cvt_pk_bf16_f32 v66, v60, v61\n\t"
      "v_cvt_pk_bf16_f32 v67, v62, v63\n\t"
      "v_perm_b32 v4, v148, v144, s87\n\t"
      "v_perm_b32 v5, v156, v152, s87\n\t"
      "v_mfma_f32_16x16x32_bf16 v[52:55], v[100:103], v[240:243], v[52:55]\n\t"
      "ds_write_b128 v252, v[64:67] offset:11264\n\t"
      "v_mfma_f32_16x16x32_bf16 v[56:59], v[104:107], v[240:243], 0\n\t"
      "v_perm_b32 v6, v164, v160, s87\n\t"
      "v_perm_b32 v7, v172, v168, s87\n\t"
      "v_perm_b32 v34, v180, v176, s87\n\t"
      "v_perm_b32 v35, v188, v184, s87\n\t"
      "v_mfma_f32_16x16x32_bf16 v[56:59], v[132:135], v[244:247], v[56:59]\n\t"
      "v_cvt_pk_bf16_f32 v68, v48, v49\n\t"
      "v_cvt_pk_bf16_f32 v69, v50, v51\n\t"
      "v_perm_b32 v8, v149, v145, s86\n\t"
      "v_perm_b32 v9, v157, v153, s86\n\t"
      "v_mfma_f32_16x16x32_bf16 v[60:63], v[132:135], v[246:249], 0\n\t"
      "v_cvt_pk_bf16_f32 v70, v52, v53\n\t"
      "v_cvt_pk_bf16_f32 v71, v54, v55\n\t"
      "v_perm_b32 v10, v165, v161, s86\n\t"
      "v_perm_b32 v11, v173, v169, s86\n\t"
      "v_mfma_f32_16x16x32_bf16 v[60:63], v[108:111], v[240:243], v[60:63]\n\t"
      "ds_write_b128 v252, v[68:71] offset:16384\n\t"
      "v_mfma_f32_16x16x32_bf16 v[48:51], v[112:115], v[240:243], 0\n\t"
      "v_perm_b32 v36, v181, v177, s86\n\t"
      "v_perm_b32 v37, v189, v185, s86\n\t"
      "v_perm_b32 v12, v149, v145, s87\n\t"
      "v_perm_b32 v13, v157, v153, s87\n\t"
      "v_mfma_f32_16x16x32_bf16 v[48:51], v[136:139], v[244:247], v[48:51]\n\t"
      "v_cvt_pk_bf16_f32 v64, v56, v57\n\t"
      "v_cvt_pk_bf16_f32 v65, v58, v59\n\t"
      "v_perm_b32 v14, v165, v161, s87\n\t"
      "v_perm_b32 v15, v173, v169, s87\n\t"
      "v_mfma_f32_16x16x32_bf16 v[52:55], v[136:139], v[246:249], 0\n\t"
      "v_cvt_pk_bf16_f32 v66, v60, v61\n\t"
      "v_cvt_pk_bf16_f32 v67, v62, v63\n\t"
      "v_perm_b32 v38, v181, v177, s87\n\t"
      "v_perm_b32 v39, v189, v185, s87\n\t"
      "v_mfma_f32_16x16x32_bf16 v[52:55], v[116:119], v[240:243], v[52:55]\n\t"
      "ds_write_b128 v252, v[64:67] offset:17408\n\t"
      "v_mfma_f32_16x16x32_bf16 v[56:59], v[120:123], v[240:243], 0\n\t"
      "v_perm_b32 v16, v150, v146, s86\n\t"
      "v_perm_b32 v17, v158, v154, s86\n\t"
      "v_perm_b32 v18, v166, v162, s86\n\t"
      "v_perm_b32 v19, v174, v170, s86\n\t"
      "v_mfma_f32_16x16x32_bf16 v[56:59], v[140:143], v[244:247], v[56:59]\n\t"
      "v_cvt_pk_bf16_f32 v68, v48, v49\n\t"
      "v_cvt_pk_bf16_f32 v69, v50, v51\n\t"
      "v_perm_b32 v40, v182, v178, s86\n\t"
      "v_perm_b32 v41, v190, v186, s86\n\t"
      "v_mfma_f32_16x16x32_bf16 v[60:63], v[140:143], v[246:249], 0\n\t"
      "v_cvt_pk_bf16_f32 v70, v52, v53\n\t"
      "v_cvt_pk_bf16_f32 v71, v54, v55\n\t"
      "v_perm_b32 v20, v150, v146, s87\n\t"
      "v_perm_b32 v21, v158, v154, s87\n\t"
      "v_mfma_f32_16x16x32_bf16 v[60:63], v[124:127], v[240:243], v[60:63]\n\t"
      "ds_write_b128 v252, v[68:71] offset:18432\n\t"
      "v_mfma_f32_16x16x32_bf16 v[48:51], v[0:3], v[216:219], 0\n\t"
      "v_perm_b32 v22, v166, v162, s87\n\t"
      "v_perm_b32 v23, v174, v170, s87\n\t"
      "v_perm_b32 v42, v182, v178, s87\n\t"
      "v_perm_b32 v43, v190, v186, s87\n\t"
      "v_mfma_f32_16x16x32_bf16 v[48:51], v[32:35], v[220:223], v[48:51]\n\t"
      "v_cvt_pk_bf16_f32 v64, v56, v57\n\t"
      "v_cvt_pk_bf16_f32 v65, v58, v59\n\t"
      "v_perm_b32 v24, v151, v147, s86\n\t"
      "v_perm_b32 v25, v159, v155, s86\n\t"
      "v_mfma_f32_16x16x32_bf16 v[52:55], v[32:35], v[222:225], 0\n\t"
      "v_cvt_pk_bf16_f32 v66, v60, v61\n\t"
      "v_cvt_pk_bf16_f32 v67, v62, v63\n\t"
      "v_perm_b32 v26, v167, v163, s86\n\t"
      "v_perm_b32 v27, v175, v171, s86\n\t"
      "v_mfma_f32_16x16x32_bf16 v[52:55], v[4:7], v[216:219], v[52:55]\n\t"
      "ds_write_b128 v252, v[64:67] offset:19456\n\t"
      "v_mfma_f32_16x16x32_bf16 v[56:59], v[8:11], v[216:219], 0\n\t"
      "v_perm_b32 v44, v183, v179, s86\n\t"
      "v_perm_b32 v45, v191, v187, s86\n\t"
      "v_perm_b32 v28, v151, v147, s87\n\t"
      "v_mfma_f32_16x16x32_bf16 v[56:59], v[36:39], v[220:223], v[56:59]\n\t"
      "v_cvt_pk_bf16_f32 v68, v48, v49\n\t"
      "v_cvt_pk_bf16_f32 v69, v50, v51\n\t"
      "v_perm_b32 v29, v159, v155, s87\n\t"
      "v_mfma_f32_16x16x32_bf16 v[60:63], v[36:39], v[222:225], 0\n\t"
      "v_cvt_pk_bf16_f32 v70, v52, v53\n\t"
      "v_cvt_pk_bf16_f32 v71, v54, v55\n\t"
      "v_perm_b32 v30, v167, v163, s87\n\t"
      "v_mfma_f32_16x16x32_bf16 v[60:63], v[12:15], v[216:219], v[60:63]\n\t"
      "ds_write_b128 v252, v[68:71] offset:4096\n\t"
      "s_waitcnt lgkmcnt(1)\n\t"
      "s_barrier\n\t"
      "v_mfma_f32_16x16x32_bf16 v[48:51], v[16:19], v[216:219], 0\n\t"
      "v_perm_b32 v31, v175, v171, s87\n\t"
      "v_perm_b32 v46, v183, v179, s87\n\t"
      "v_perm_b32 v47, v191, v187, s87\n\t"
      "v_mfma_f32_16x16x32_bf16 v[48:51], v[40:43], v[220:223], v[48:51]\n\t"
      "v_cvt_pk_bf16_f32 v64, v56, v57\n\t"
      "v_cvt_pk_bf16_f32 v65, v58, v59\n\t"
      "v_mfma_f32_16x16x32_bf16 v[52:55], v[40:43], v[222:225], 0\n\t"
      "v_cvt_pk_bf16_f32 v66, v60, v61\n\t"
      "v_cvt_pk_bf16_f32 v67, v62, v63\n\t"
      "v_mfma_f32_16x16x32_bf16 v[52:55], v[20:23], v[216:219], v[52:55]\n\t"
      "ds_write_b128 v252, v[64:67] offset:5120\n\t"
      "v_mfma_f32_16x16x32_bf16 v[56:59], v[24:27], v[216:219], 0\n\t"
      "v_mfma_f32_16x16x32_bf16 v[56:59], v[44:47], v[220:223], v[56:59]\n\t"
      "v_cvt_pk_bf16_f32 v68, v48, v49\n\t"
      "v_cvt_pk_bf16_f32 v69, v50, v51\n\t"
      "v_mfma_f32_16x16x32_bf16 v[60:63], v[44:47], v[222:225], 0\n\t"
      "s_nop 1\n\t"
      "v_cvt_pk_bf16_f32 v70, v52, v53\n\t"
      "v_cvt_pk_bf16_f32 v71, v54, v55\n\t"
      "v_mfma_f32_16x16x32_bf16 v[60:63], v[28:31], v[216:219], v[60:63]\n\t"
      "ds_write_b128 v252, v[68:71] offset:6144\n\t"
      "v_mfma_f32_16x16x32_bf16 v[48:51], v[0:3], v[228:231], 0\n\t"
      "v_mfma_f32_16x16x32_bf16 v[48:51], v[32:35], v[232:235], v[48:51]\n\t"
      "v_cvt_pk_bf16_f32 v64, v56, v57\n\t"
      "v_cvt_pk_bf16_f32 v65, v58, v59\n\t"
      "v_mfma_f32_16x16x32_bf16 v[52:55], v[32:35], v[234:237], 0\n\t"
      "s_nop 1\n\t"
      "v_cvt_pk_bf16_f32 v66, v60, v61\n\t"
      "v_cvt_pk_bf16_f32 v67, v62, v63\n\t"
      "v_mfma_f32_16x16x32_bf16 v[52:55], v[4:7], v[228:231], v[52:55]\n\t"
      "ds_write_b128 v252, v[64:67] offset:7168\n\t"
      "v_mfma_f32_16x16x32_bf16 v[56:59], v[8:11], v[228:231], 0\n\t"
      "v_mfma_f32_16x16x32_bf16 v[56:59], v[36:39], v[232:235], v[56:59]\n\t"
      "v_cvt_pk_bf16_f32 v68, v48, v49\n\t"
      "v_cvt_pk_bf16_f32 v69, v50, v51\n\t"
      "v_mfma_f32_16x16x32_bf16 v[60:63], v[36:39], v[234:237], 0\n\t"
      "s_nop 1\n\t"
      "v_cvt_pk_bf16_f32 v70, v52, v53\n\t"
      "v_cvt_pk_bf16_f32 v71, v54, v55\n\t"
      "v_mfma_f32_16x16x32_bf16 v[60:63], v[12:15], v[228:231], v[60:63]\n\t"
      "ds_write_b128 v252, v[68:71] offset:12288\n\t"
      "v_mfma_f32_16x16x32_bf16 v[48:51], v[16:19], v[228:231], 0\n\t"
      "v_mfma_f32_16x16x32_bf16 v[48:51], v[40:43], v[232:235], v[48:51]\n\t"
      "v_cvt_pk_bf16_f32 v64, v56, v57\n\t"
      "v_cvt_pk_bf16_f32 v65, v58, v59\n\t"
      "v_mfma_f32_16x16x32_bf16 v[52:55], v[40:43], v[234:237], 0\n\t"
      "s_nop 1\n\t"
      "v_cvt_pk_bf16_f32 v66, v60, v61\n\t"
      "v_cvt_pk_bf16_f32 v67, v62, v63\n\t"
      "v_mfma_f32_16x16x32_bf16 v[52:55], v[20:23], v[228:231], v[52:55]\n\t"
      "ds_write_b128 v252, v[64:67] offset:13312\n\t"
      "v_mfma_f32_16x16x32_bf16 v[56:59], v[24:27], v[228:231], 0\n\t"
      "v_mfma_f32_16x16x32_bf16 v[56:59], v[44:47], v[232:235], v[56:59]\n\t"
      "v_cvt_pk_bf16_f32 v68, v48, v49\n\t"
      "v_cvt_pk_bf16_f32 v69, v50, v51\n\t"
      "v_mfma_f32_16x16x32_bf16 v[60:63], v[44:47], v[234:237], 0\n\t"
      "s_nop 1\n\t"
      "v_cvt_pk_bf16_f32 v70, v52, v53\n\t"
      "v_cvt_pk_bf16_f32 v71, v54, v55\n\t"
      "v_mfma_f32_16x16x32_bf16 v[60:63], v[28:31], v[228:231], v[60:63]\n\t"
      "ds_write_b128 v252, v[68:71] offset:14336\n\t"
      "v_mfma_f32_16x16x32_bf16 v[48:51], v[0:3], v[240:243], 0\n\t"
      "v_mfma_f32_16x16x32_bf16 v[48:51], v[32:35], v[244:247], v[48:51]\n\t"
      "v_cvt_pk_bf16_f32 v64, v56, v57\n\t"
      "v_cvt_pk_bf16_f32 v65, v58, v59\n\t"
      "v_mfma_f32_16x16x32_bf16 v[52:55], v[32:35], v[246:249], 0\n\t"
      "s_nop 1\n\t"
      "v_cvt_pk_bf16_f32 v66, v60, v61\n\t"
      "v_cvt_pk_bf16_f32 v67, v62, v63\n\t"
      "v_mfma_f32_16x16x32_bf16 v[52:55], v[4:7], v[240:243], v[52:55]\n\t"
      "ds_write_b128 v252, v[64:67] offset:15360\n\t"
      "v_mfma_f32_16x16x32_bf16 v[56:59], v[8:11], v[240:243], 0\n\t"
      "v_mfma_f32_16x16x32_bf16 v[56:59], v[36:39], v[244:247], v[56:59]\n\t"
      "v_cvt_pk_bf16_f32 v68, v48, v49\n\t"
      "v_cvt_pk_bf16_f32 v69, v50, v51\n\t"
      "v_mfma_f32_16x16x32_bf16 v[60:63], v[36:39], v[246:249], 0\n\t"
      "s_nop 1\n\t"
      "v_cvt_pk_bf16_f32 v70, v52, v53\n\t"
      "v_cvt_pk_bf16_f32 v71, v54, v55\n\t"
      "v_mfma_f32_16x16x32_bf16 v[60:63], v[12:15], v[240:243], v[60:63]\n\t"
      "ds_write_b128 v252, v[68:71] offset:20480\n\t"
      "s_waitcnt lgkmcnt(1)\n\t"
      "s_barrier\n\t"
      "v_mfma_f32_16x16x32_bf16 v[48:51], v[16:19], v[240:243], 0\n\t"
      "v_mfma_f32_16x16x32_bf16 v[48:51], v[40:43], v[244:247], v[48:51]\n\t"
      "v_cvt_pk_bf16_f32 v64, v56, v57\n\t"
      "v_cvt_pk_bf16_f32 v65, v58, v59\n\t"
      "v_mfma_f32_16x16x32_bf16 v[52:55], v[40:43], v[246:249], 0\n\t"
      "v_cvt_pk_bf16_f32 v66, v60, v61\n\t"
      "v_cvt_pk_bf16_f32 v67, v62, v63\n\t"
      "v_mfma_f32_16x16x32_bf16 v[52:55], v[20:23], v[240:243], v[52:55]\n\t"
      "ds_write_b128 v252, v[64:67] offset:21504\n\t"
      "v_mfma_f32_16x16x32_bf16 v[56:59], v[24:27], v[240:243], 0\n\t"
      "v_mfma_f32_16x16x32_bf16 v[56:59], v[44:47], v[244:247], v[56:59]\n\t"
      "v_cvt_pk_bf16_f32 v68, v48, v49\n\t"
      "v_cvt_pk_bf16_f32 v69, v50, v51\n\t"
      "v_mfma_f32_16x16x32_bf16 v[60:63], v[44:47], v[246:249], 0\n\t"
      "s_nop 1\n\t"
      "v_cvt_pk_bf16_f32 v70, v52, v53\n\t"
      "v_cvt_pk_bf16_f32 v71, v54, v55\n\t"
      "v_mfma_f32_16x16x32_bf16 v[60:63], v[28:31], v[240:243], v[60:63]\n\t"
      "ds_write_b128 v252, v[68:71] offset:22528\n\t"
      "v_cvt_pk_bf16_f32 v64, v56, v57\n\t"
      "v_cvt_pk_bf16_f32 v65, v58, v59\n\t"
      "s_nop 4\n\t"
      "v_cvt_pk_bf16_f32 v66, v60, v61\n\t"
      "v_cvt_pk_bf16_f32 v67, v62, v63\n\t"
      "ds_write_b128 v252, v[64:67] offset:23552\n\t"
      "s_waitcnt lgkmcnt(0)\n\t"
      "s_barrier\n\t"
      "s_setprio 0\n\t"
      "s_mov_b32 s91, 1\n\t"
      "v_mbcnt_lo_u32_b32 v94, -1, 0\n\t"
      "v_mbcnt_hi_u32_b32 v94, -1, v94\n\t"
      "v_lshrrev_b32 v96, 3, v94\n\t"
      "v_mul_u32_u24 v96, 896, v96\n\t"
      "v_and_b32 v88, 7, v94\n\t"
      "v_lshl_add_u32 v96, v88, 4, v96\n\t"
      "v_add_u32 v97, 7168, v96\n\t"
      "v_add_u32 v98, 14336, v96\n\t"
      "v_add_u32 v99, 21504, v96\n\t"
      "v_add_u32 v100, 28672, v96\n\t"
      "v_add_u32 v101, 35840, v96\n\t"
      "v_add_u32 v102, 43008, v96\n\t"
      "v_add_u32 v103, 50176, v96\n\t"
      "s_mov_b32 s60, %[j0]\n\t"
      "s_mov_b32 s61, %[j1]\n\t"
      "s_mov_b32 s62, %[j2]\n\t"
      "s_mov_b32 s63, %[j3]\n\t"
      "s_mov_b32 s64, %[j4]\n\t"
      "s_mov_b32 s65, 22937600\n\t"
      "s_mov_b32 s66, %[due]\n\t"
      "s_mov_b32 s67, 0\n\t"
      "s_mov_b32 s70, 13\n\t"
      "s_mov_b64 s[84:85], %[part]\n\t"
      "s_and_b32 s85, s85, 0xffff\n\t"
      "s_mov_b32 s86, 22937600\n\t"
      "s_mov_b32 s87, 0x20000\n\t"
      "s_mov_b64 s[68:69], %[part]\n\t"
      "s_mov_b32 s71, 3\n\t"
      "s_mov_b32 s90, %[g0]\n\t"
      "s_mov_b32 s89, 0x200000\n\t"
      "s_barrier\n\t"
      "s_cmp_eq_u32 %[ld], 0\n\t"
      "s_cbranch_scc1 40f\n\t"
      "s_and_b32 s78, s71, 3\n\t"
      "s_mul_i32 s79, s78, 114688\n\t"
      "s_lshl_b32 s80, s90, 7\n\t"
      "s_add_u32 s79, s79, s80\n\t"
      "s_mov_b64 s[72:73], %[wo]\n\t"
      "s_add_u32 s72, s72, s79\n\t"
      "s_addc_u32 s73, s73, 0\n\t"
      "s_and_b32 s79, s71, 7\n\t"
      "s_mul_i32 s79, s79, 16896\n\t"
      "s_sub_u32 m0, %[od], s79\n\t"
      "s_nop 0\n\t"
      "global_load_lds_dwordx4 v96, s[72:73]\n\t"
      "s_add_u32 m0, m0, 1056\n\t"
      "s_nop 0\n\t"
      "global_load_lds_dwordx4 v97, s[72:73]\n\t"
      "s_add_u32 m0, m0, 1056\n\t"
      "s_nop 0\n\t"
      "global_load_lds_dwordx4 v98, s[72:73]\n\t"
      "s_add_u32 m0, m0, 1056\n\t"
      "s_nop 0\n\t"
      "global_load_lds_dwordx4 v99, s[72:73]\n\t"
      "s_add_u32 m0, m0, 1056\n\t"
      "s_nop 0\n\t"
      "global_load_lds_dwordx4 v100, s[72:73]\n\t"
      "s_add_u32 m0, m0, 1056\n\t"
      "s_nop 0\n\t"
      "global_load_lds_dwordx4 v101, s[72:73]\n\t"
      "s_add_u32 m0, m0, 1056\n\t"
      "s_nop 0\n\t"
      "global_load_lds_dwordx4 v102, s[72:73]\n\t"
      "s_add_u32 m0, m0, 1056\n\t"
      "s_nop 0\n\t"
      "global_load_lds_dwordx4 v103, s[72:73]\n\t"
      "s_add_u32 s71, s71, 1\n\t"
      "s_cmp_eq_u32 s78, 3\n\t"
      "s_addc_u32 s90, s90, 0\n\t"
      "s_cmp_eq_u32 s90, 7\n\t"
      "s_cselect_b32 s90, 0, s90\n\t"
      "s_and_b32 s78, s71, 3\n\t"
      "s_mul_i32 s79, s78, 114688\n\t"
      "s_lshl_b32 s80, s90, 7\n\t"
      "s_add_u32 s79, s79, s80\n\t"
      "s_mov_b64 s[72:73], %[wo]\n\t"
      "s_add_u32 s72, s72, s79\n\t"
      "s_addc_u32 s73, s73, 0\n\t"
      "s_and_b32 s79, s71, 7\n\t"
      "s_mul_i32 s79, s79, 16896\n\t"
      "s_sub_u32 m0, %[od], s79\n\t"
      "s_nop 0\n\t"
      "global_load_lds_dwordx4 v96, s[72:73]\n\t"
      "s_add_u32 m0, m0, 1056\n\t"
      "s_nop 0\n\t"
      "global_load_lds_dwordx4 v97, s[72:73]\n\t"
      "s_add_u32 m0, m0, 1056\n\t"
      "s_nop 0\n\t"
      "global_load_lds_dwordx4 v98, s[72:73]\n\t"
      "s_add_u32 m0, m0, 1056\n\t"
      "s_nop 0\n\t"
      "global_load_lds_dwordx4 v99, s[72:73]\n\t"
      "s_add_u32 m0, m0, 1056\n\t"
      "s_nop 0\n\t"
      "global_load_lds_dwordx4 v100, s[72:73]\n\t"
      "s_add_u32 m0, m0, 1056\n\t"
      "s_nop 0\n\t"
      "global_load_lds_dwordx4 v101, s[72:73]\n\t"
      "s_add_u32 m0, m0, 1056\n\t"
      "s_nop 0\n\t"
      "global_load_lds_dwordx4 v102, s[72:73]\n\t"
      "s_add_u32 m0, m0, 1056\n\t"
      "s_nop 0\n\t"
      "global_load_lds_dwordx4 v103, s[72:73]\n\t"
      "s_add_u32 s71, s71, 1\n\t"
      "s_cmp_eq_u32 s78, 3\n\t"
      "s_addc_u32 s90, s90, 0\n\t"
      "s_cmp_eq_u32 s90, 7\n\t"
      "s_cselect_b32 s90, 0, s90\n\t"
      "40:\n\t"
      "v_mov_b32 v88, 80\n\t"
      "ds_read_b64 v[90:91], v88\n\t"
      "v_lshlrev_b32 v92, 4, v94\n\t"
      "v_lshrrev_b32 v93, 4, v94\n\t"
      "v_lshlrev_b32 v93, 5, v93\n\t"
      "s_waitcnt lgkmcnt(0)\n\t"
      "v_readfirstlane_b32 s82, v90\n\t"
      "v_readfirstlane_b32 s83, v91\n\t"
      "v_bfe_u32 v88, v94, 2, 2\n\t"
      "v_lshrrev_b32 v89, 4, v94\n\t"
      "v_cmp_eq_u32 vcc, v88, v89\n\t"
      "v_and_b32 v88, 1, v94\n\t"
      "v_lshlrev_b32 v88, 4, v88\n\t"
      "v_mov_b32 v89, 0x3f80\n\t"
      "v_lshlrev_b32 v88, v88, v89\n\t"
      "v_cndmask_b32 v88, 0, v88, vcc\n\t"
      "v_and_b32 v89, 2, v94\n\t"
      "v_cmp_eq_u32 vcc, 0, v89\n\t"
      "v_mov_b64 v[82:83], 0\n\t"
      "v_mov_b64 v[84:85], 0\n\t"
      "v_cndmask_b32 v80, 0, v88, vcc\n\t"
      "v_cndmask_b32 v81, v88, 0, vcc\n\t"
      "v_mov_b64 v[86:87], v[80:81]\n\t"
      "s_nop 1\n\t"
      "v_mov_b64 v[32:33], 0\n\t"
      "v_mov_b64 v[34:35], 0\n\t"
      "v_mov_b64 v[36:37], 0\n\t"
      "v_mov_b64 v[38:39], 0\n\t"
      "v_mov_b64 v[40:41], 0\n\t"
      "v_mov_b64 v[42:43], 0\n\t"
      "v_mov_b64 v[44:45], 0\n\t"
      "v_mov_b64 v[46:47], 0\n\t"
      "v_mov_b64 v[48:49], 0\n\t"
      "v_mov_b64 v[50:51], 0\n\t"
      "v_mov_b64 v[52:53], 0\n\t"
      "v_mov_b64 v[54:55], 0\n\t"
      "v_mov_b64 v[56:57], 0\n\t"
      "v_mov_b64 v[58:59], 0\n\t"
      "v_mov_b64 v[60:61], 0\n\t"
      "v_mov_b64 v[62:63], 0\n\t"
      "v_mov_b64 v[64:65], 0\n\t"
      "v_mov_b64 v[66:67], 0\n\t"
      "v_mov_b64 v[68:69], 0\n\t"
      "v_mov_b64 v[70:71], 0\n\t"
      "v_mov_b64 v[72:73], 0\n\t"
      "v_mov_b64 v[74:75], 0\n\t"
      "v_mov_b64 v[76:77], 0\n\t"
      "v_mov_b64 v[78:79], 0\n\t"
      "s_cmp_eq_u32 %[ld], 0\n\t"
      "s_cbranch_scc1 50f\n\t"
      "s_waitcnt vmcnt(8)\n\t"
      "s_barrier\n\t"
      "s_and_b32 s78, s71, 3\n\t"
      "s_mul_i32 s79, s78, 114688\n\t"
      "s_lshl_b32 s80, s90, 7\n\t"
      "s_add_u32 s79, s79, s80\n\t"
      "s_mov_b64 s[72:73], %[wo]\n\t"
      "s_add_u32 s72, s72, s79\n\t"
      "s_addc_u32 s73, s73, 0\n\t"
      "s_and_b32 s79, s71, 7\n\t"
      "s_mul_i32 s79, s79, 16896\n\t"
      "s_sub_u32 m0, %[od], s79\n\t"
      "s_nop 0\n\t"
      "global_load_lds_dwordx4 v96, s[72:73]\n\t"
      "s_add_u32 m0, m0, 1056\n\t"
      "s_nop 0\n\t"
      "global_load_lds_dwordx4 v97, s[72:73]\n\t"
      "s_add_u32 m0, m0, 1056\n\t"
      "s_nop 0\n\t"
      "global_load_lds_dwordx4 v98, s[72:73]\n\t"
      "s_add_u32 m0, m0, 1056\n\t"
      "s_nop 0\n\t"
      "global_load_lds_dwordx4 v99, s[72:73]\n\t"
      "s_add_u32 m0, m0, 1056\n\t"
      "s_nop 0\n\t"
      "global_load_lds_dwordx4 v100, s[72:73]\n\t"
      "s_add_u32 m0, m0, 1056\n\t"
      "s_nop 0\n\t"
      "global_load_lds_dwordx4 v101, s[72:73]\n\t"
      "s_add_u32 m0, m0, 1056\n\t"
      "s_nop 0\n\t"
      "global_load_lds_dwordx4 v102, s[72:73]\n\t"
      "s_add_u32 m0, m0, 1056\n\t"
      "s_nop 0\n\t"
      "global_load_lds_dwordx4 v103, s[72:73]\n\t"
      "s_add_u32 s71, s71, 1\n\t"
      "s_cmp_eq_u32 s78, 3\n\t"
      "s_addc_u32 s90, s90, 0\n\t"
      "s_cmp_eq_u32 s90, 7\n\t"
      "s_cselect_b32 s90, 0, s90\n\t"
      "41:\n\t"
      "s_and_b32 s78, s71, 3\n\t"
      "s_mul_i32 s79, s78, 114688\n\t"
      "s_lshl_b32 s80, s90, 7\n\t"
      "s_add_u32 s79, s79, s80\n\t"
      "s_mov_b64 s[72:73], %[wo]\n\t"
      "s_add_u32 s72, s72, s79\n\t"
      "s_addc_u32 s73, s73, 0\n\t"
      "s_and_b32 s79, s71, 7\n\t"
      "s_mul_i32 s79, s79, 16896\n\t"
      "s_sub_u32 m0, %[od], s79\n\t"
      "s_nop 0\n\t"
      "global_load_lds_dwordx4 v96, s[72:73]\n\t"
      "s_add_u32 m0, m0, 1056\n\t"
      "s_nop 0\n\t"
      "global_load_lds_dwordx4 v97, s[72:73]\n\t"
      "s_add_u32 m0, m0, 1056\n\t"
      "s_nop 0\n\t"
      "global_load_lds_dwordx4 v98, s[72:73]\n\t"
      "s_add_u32 m0, m0, 1056\n\t"
      "s_nop 0\n\t"
      "global_load_lds_dwordx4 v99, s[72:73]\n\t"
      "s_add_u32 m0, m0, 1056\n\t"
      "s_nop 0\n\t"
      "global_load_lds_dwordx4 v100, s[72:73]\n\t"
      "s_add_u32 m0, m0, 1056\n\t"
      "s_nop 0\n\t"
      "global_load_lds_dwordx4 v101, s[72:73]\n\t"
      "s_add_u32 m0, m0, 1056\n\t"
      "s_nop 0\n\t"
      "global_load_lds_dwordx4 v102, s[72:73]\n\t"
      "s_add_u32 m0, m0, 1056\n\t"
      "s_nop 0\n\t"
      "global_load_lds_dwordx4 v103, s[72:73]\n\t"
      "s_add_u32 s71, s71, 1\n\t"
      "s_cmp_eq_u32 s78, 3\n\t"
      "s_addc_u32 s90, s90, 0\n\t"
      "s_cmp_eq_u32 s90, 7\n\t"
      "s_cselect_b32 s90, 0, s90\n\t"
      "s_and_b32 s78, s71, 3\n\t"
      "s_mul_i32 s79, s78, 114688\n\t"
      "s_lshl_b32 s80, s90, 7\n\t"
      "s_add_u32 s79, s79, s80\n\t"
      "s_mov_b64 s[72:73], %[wo]\n\t"
      "s_add_u32 s72, s72, s79\n\t"
      "s_addc_u32 s73, s73, 0\n\t"
      "s_and_b32 s79, s71, 7\n\t"
      "s_mul_i32 s79, s79, 16896\n\t"
      "s_sub_u32 m0, %[od], s79\n\t"
      "s_nop 0\n\t"
      "global_load_lds_dwordx4 v96, s[72:73]\n\t"
      "s_add_u32 m0, m0, 1056\n\t"
      "s_nop 0\n\t"
      "global_load_lds_dwordx4 v97, s[72:73]\n\t"
      "s_add_u32 m0, m0, 1056\n\t"
      "s_nop 0\n\t"
      "global_load_lds_dwordx4 v98, s[72:73]\n\t"
      "s_add_u32 m0, m0, 1056\n\t"
      "s_nop 0\n\t"
      "global_load_lds_dwordx4 v99, s[72:73]\n\t"
      "s_add_u32 m0, m0, 1056\n\t"
      "s_nop 0\n\t"
      "global_load_lds_dwordx4 v100, s[72:73]\n\t"
      "s_add_u32 m0, m0, 1056\n\t"
      "s_nop 0\n\t"
      "global_load_lds_dwordx4 v101, s[72:73]\n\t"
      "s_add_u32 m0, m0, 1056\n\t"
      "s_nop 0\n\t"
      "global_load_lds_dwordx4 v102, s[72:73]\n\t"
      "s_add_u32 m0, m0, 1056\n\t"
      "s_nop 0\n\t"
      "global_load_lds_dwordx4 v103, s[72:73]\n\t"
      "s_add_u32 s71, s71, 1\n\t"
      "s_cmp_eq_u32 s78, 3\n\t"
      "s_addc_u32 s90, s90, 0\n\t"
      "s_cmp_eq_u32 s90, 7\n\t"
      "s_cselect_b32 s90, 0, s90\n\t"
      "s_waitcnt vmcnt(16)\n\t"
      "s_cmp_eq_u32 s71, 28\n\t"
      "s_cbranch_scc1 42f\n\t"
      "s_barrier\n\t"
      "s_branch 41b\n\t"
      "42:\n\t"
      "s_mov_b32 s70, 2\n\t"
      "50:\n\t"
      "s_bitcmp1_b32 s67, 2\n\t"
      "s_cbranch_scc0 64f\n\t"
      "v_lshlrev_b32 v136, 16, v112\n\t"
      "v_and_b32 v137, 0xffff0000, v112\n\t"
      "v_add_f32 v32, v32, v136\n\t"
      "v_add_f32 v33, v33, v137\n\t"
      "v_lshlrev_b32 v138, 16, v113\n\t"
      "v_and_b32 v139, 0xffff0000, v113\n\t"
      "v_add_f32 v34, v34, v138\n\t"
      "v_add_f32 v35, v35, v139\n\t"
      "v_lshlrev_b32 v140, 16, v114\n\t"
      "v_and_b32 v141, 0xffff0000, v114\n\t"
      "v_add_f32 v36, v36, v140\n\t"
      "v_add_f32 v37, v37, v141\n\t"
      "v_lshlrev_b32 v142, 16, v115\n\t"
      "v_and_b32 v143, 0xffff0000, v115\n\t"
      "v_add_f32 v38, v38, v142\n\t"
      "v_add_f32 v39, v39, v143\n\t"
      "v_lshlrev_b32 v136, 16, v116\n\t"
      "v_and_b32 v137, 0xffff0000, v116\n\t"
      "v_add_f32 v40, v40, v136\n\t"
      "v_add_f32 v41, v41, v137\n\t"
      "v_lshlrev_b32 v138, 16, v117\n\t"
      "v_and_b32 v139, 0xffff0000, v117\n\t"
      "v_add_f32 v42, v42, v138\n\t"
      "v_add_f32 v43, v43, v139\n\t"
      "v_lshlrev_b32 v140, 16, v118\n\t"
      "v_and_b32 v141, 0xffff0000, v118\n\t"
      "v_add_f32 v44, v44, v140\n\t"
      "v_add_f32 v45, v45, v141\n\t"
      "v_lshlrev_b32 v142, 16, v119\n\t"
      "v_and_b32 v143, 0xffff0000, v119\n\t"
      "v_add_f32 v46, v46, v142\n\t"
      "v_add_f32 v47, v47, v143\n\t"
      "v_lshlrev_b32 v136, 16, v120\n\t"
      "v_and_b32 v137, 0xffff0000, v120\n\t"
      "v_add_f32 v48, v48, v136\n\t"
      "v_add_f32 v49, v49, v137\n\t"
      "v_lshlrev_b32 v138, 16, v121\n\t"
      "v_and_b32 v139, 0xffff0000, v121\n\t"
      "v_add_f32 v50, v50, v138\n\t"
      "v_add_f32 v51, v51, v139\n\t"
      "v_lshlrev_b32 v140, 16, v122\n\t"
      "v_and_b32 v141, 0xffff0000, v122\n\t"
      "v_add_f32 v52, v52, v140\n\t"
      "v_add_f32 v53, v53, v141\n\t"
      "v_lshlrev_b32 v142, 16, v123\n\t"
      "v_and_b32 v143, 0xffff0000, v123\n\t"
      "v_add_f32 v54, v54, v142\n\t"
      "v_add_f32 v55, v55, v143\n\t"
      "v_lshlrev_b32 v136, 16, v124\n\t"
      "v_and_b32 v137, 0xffff0000, v124\n\t"
      "v_add_f32 v56, v56, v136\n\t"
      "v_add_f32 v57, v57, v137\n\t"
      "v_lshlrev_b32 v138, 16, v125\n\t"
      "v_and_b32 v139, 0xffff0000, v125\n\t"
      "v_add_f32 v58, v58, v138\n\t"
      "v_add_f32 v59, v59, v139\n\t"
      "v_lshlrev_b32 v140, 16, v126\n\t"
      "v_and_b32 v141, 0xffff0000, v126\n\t"
      "v_add_f32 v60, v60, v140\n\t"
      "v_add_f32 v61, v61, v141\n\t"
      "v_lshlrev_b32 v142, 16, v127\n\t"
      "v_and_b32 v143, 0xffff0000, v127\n\t"
      "v_add_f32 v62, v62, v142\n\t"
      "v_add_f32 v63, v63, v143\n\t"
      "v_lshlrev_b32 v136, 16, v128\n\t"
      "v_and_b32 v137, 0xffff0000, v128\n\t"
      "v_add_f32 v64, v64, v136\n\t"
      "v_add_f32 v65, v65, v137\n\t"
      "v_lshlrev_b32 v138, 16, v129\n\t"
      "v_and_b32 v139, 0xffff0000, v129\n\t"
      "v_add_f32 v66, v66, v138\n\t"
      "v_add_f32 v67, v67, v139\n\t"
      "v_lshlrev_b32 v140, 16, v130\n\t"
      "v_and_b32 v141, 0xffff0000, v130\n\t"
      "v_add_f32 v68, v68, v140\n\t"
      "v_add_f32 v69, v69, v141\n\t"
      "v_lshlrev_b32 v142, 16, v131\n\t"
      "v_and_b32 v143, 0xffff0000, v131\n\t"
      "v_add_f32 v70, v70, v142\n\t"
      "v_add_f32 v71, v71, v143\n\t"
      "v_lshlrev_b32 v136, 16, v132\n\t"
      "v_and_b32 v137, 0xffff0000, v132\n\t"
      "v_add_f32 v72, v72, v136\n\t"
      "v_add_f32 v73, v73, v137\n\t"
      "v_lshlrev_b32 v138, 16, v133\n\t"
      "v_and_b32 v139, 0xffff0000, v133\n\t"
      "v_add_f32 v74, v74, v138\n\t"
      "v_add_f32 v75, v75, v139\n\t"
      "v_lshlrev_b32 v140, 16, v134\n\t"
      "v_and_b32 v141, 0xffff0000, v134\n\t"
      "v_add_f32 v76, v76, v140\n\t"
      "v_add_f32 v77, v77, v141\n\t"
      "v_lshlrev_b32 v142, 16, v135\n\t"
      "v_and_b32 v143, 0xffff0000, v135\n\t"
      "v_add_f32 v78, v78, v142\n\t"
      "v_add_f32 v79, v79, v143\n\t"
      "s_bitset0_b32 s67, 2\n\t"
      "s_cmp_eq_u32 s70, 0\n\t"
      "s_cbranch_scc1 64f\n\t"
      "s_bitcmp1_b32 s67, 0\n\t"
      "s_cbranch_scc1 53f\n\t"
      "64:\n\t"
      "s_bitcmp1_b32 s67, 0\n\t"
      "s_cbranch_scc0 51f\n\t"
      "s_cmp_eq_u32 s70, 0\n\t"
      "s_cbranch_scc1 52f\n\t"
      "s_getreg_b32 s78, hwreg(HW_REG_IB_STS, 0, 4)\n\t"
      "s_cmp_lg_u32 s78, 0\n\t"
      "s_cbranch_scc1 53f\n\t"
      "52:\n\t"
      "s_waitcnt vmcnt(0)\n\t"
      "s_mov_b32 s88, 0\n\t"
      "v_add3_u32 v88, v0, v2, s82\n\t"
      "v_add3_u32 v90, v16, v18, s82\n\t"
      "v_add3_u32 v89, v1, v3, s83\n\t"
      "v_add3_u32 v91, v17, v19, s83\n\t"
      "v_add3_u32 v88, v4, v6, v88\n\t"
      "v_add3_u32 v90, v20, v22, v90\n\t"
      "v_add3_u32 v89, v5, v7, v89\n\t"
      "v_add3_u32 v91, v21, v23, v91\n\t"
      "v_add3_u32 v88, v8, v10, v88\n\t"
      "v_add3_u32 v90, v24, v26, v90\n\t"
      "v_add3_u32 v89, v9, v11, v89\n\t"
      "v_add3_u32 v91, v25, v27, v91\n\t"
      "s_nop 0\n\t"
      "v_permlane32_swap_b32 v88, v89\n\t"
      "v_permlane32_swap_b32 v90, v91\n\t"
      "v_add_u32 v88, v88, v89\n\t"
      "v_add_u32 v90, v90, v91\n\t"
      "s_nop 0\n\t"
      "v_add_u32_dpp v88, v88, v88 row_ror:8 row_mask:0xf bank_mask:0xf\n\t"
      "v_add_u32_dpp v90, v90, v90 row_ror:8 row_mask:0xf bank_mask:0xf\n\t"
      "s_nop 0\n\t"
      "v_add_u32_dpp v88, v88, v88 row_ror:4 row_mask:0xf bank_mask:0xf\n\t"
      "v_add_u32_dpp v90, v90, v90 row_ror:4 row_mask:0xf bank_mask:0xf\n\t"
      "s_nop 0\n\t"
      "v_add_u32_dpp v88, v88, v88 row_ror:2 row_mask:0xf bank_mask:0xf\n\t"
      "v_add_u32_dpp v90, v90, v90 row_ror:2 row_mask:0xf bank_mask:0xf\n\t"
      "s_nop 0\n\t"
      "v_add_u32_dpp v88, v88, v88 row_ror:1 row_mask:0xf bank_mask:0xf\n\t"
      "v_add_u32_dpp v90, v90, v90 row_ror:1 row_mask:0xf bank_mask:0xf\n\t"
      "v_xor_b32 v88, s82, v88\n\t"
      "v_xor_b32 v90, s82, v90\n\t"
      "v_cmp_ne_u32 vcc, v88, v12\n\t"
      "v_cmp_ne_u32 s[78:79], v90, v28\n\t"
      "s_or_b64 vcc, vcc, s[78:79]\n\t"
      "s_cbranch_scc0 54f\n\t"
      "s_sub_u32 s89, s89, 1\n\t"
      "s_cbranch_scc0 55f\n\t"
      "s_mov_b32 s89, 0\n\t"
      "54:\n\t"
      "s_cmp_eq_u32 s70, 0\n\t"
      "s_cbranch_scc1 62f\n\t"
      "v_mov_b64 v[112:113], v[0:1]\n\t"
      "v_mov_b64 v[114:115], v[2:3]\n\t"
      "v_mov_b64 v[116:117], v[4:5]\n\t"
      "v_mov_b64 v[118:119], v[6:7]\n\t"
      "v_mov_b64 v[120:121], v[8:9]\n\t"
      "v_mov_b64 v[122:123], v[10:11]\n\t"
      "v_mov_b64 v[124:125], v[16:17]\n\t"
      "v_mov_b64 v[126:127], v[18:19]\n\t"
      "v_mov_b64 v[128:129], v[20:21]\n\t"
      "v_mov_b64 v[130:131], v[22:23]\n\t"
      "v_mov_b64 v[132:133], v[24:25]\n\t"
      "v_mov_b64 v[134:135], v[26:27]\n\t"
      "s_bitset1_b32 s67, 2\n\t"
      "s_branch 63f\n\t"
      "62:\n\t"
      "v_mfma_f32_16x16x32_bf16 v[32:35], v[80:83], v[0:3], v[32:35]\n\t"
      "v_mfma_f32_16x16x32_bf16 v[36:39], v[84:87], v[0:3], v[36:39]\n\t"
      "v_mfma_f32_16x16x32_bf16 v[40:43], v[80:83], v[4:7], v[40:43]\n\t"
      "v_mfma_f32_16x16x32_bf16 v[44:47], v[84:87], v[4:7], v[44:47]\n\t"
      "v_mfma_f32_16x16x32_bf16 v[48:51], v[80:83], v[8:11], v[48:51]\n\t"
      "v_mfma_f32_16x16x32_bf16 v[52:55], v[84:87], v[8:11], v[52:55]\n\t"
      "v_mfma_f32_16x16x32_bf16 v[56:59], v[80:83], v[16:19], v[56:59]\n\t"
      "v_mfma_f32_16x16x32_bf16 v[60:63], v[84:87], v[16:19], v[60:63]\n\t"
      "v_mfma_f32_16x16x32_bf16 v[64:67], v[80:83], v[20:23], v[64:67]\n\t"
      "v_mfma_f32_16x16x32_bf16 v[68:71], v[84:87], v[20:23], v[68:71]\n\t"
      "v_mfma_f32_16x16x32_bf16 v[72:75], v[80:83], v[24:27], v[72:75]\n\t"
      "v_mfma_f32_16x16x32_bf16 v[76:79], v[84:87], v[24:27], v[76:79]\n\t"
      "63:\n\t"
      "s_mov_b32 s60, s61\n\t"
      "s_mov_b32 s61, s62\n\t"
      "s_mov_b32 s62, s63\n\t"
      "s_mov_b32 s63, s64\n\t"
      "s_mov_b32 s64, s65\n\t"
      "s_lshr_b32 s66, s66, 4\n\t"
      "s_bitset0_b32 s67, 0\n\t"
      "51:\n\t"
      "s_bitcmp1_b32 s67, 1\n\t"
      "s_cbranch_scc0 56f\n\t"
      "s_waitcnt lgkmcnt(0)\n\t"
      "s_bitset0_b32 s67, 1\n\t"
      "s_xor_b64 s[74:75], s[74:75], s[82:83]\n\t"
      "s_xor_b64 s[76:77], s[76:77], s[82:83]\n\t"
      "s_or_b64 s[74:75], s[74:75], s[76:77]\n\t"
      "s_cbranch_scc0 55f\n\t"
      "s_sub_u32 s89, s89, 1\n\t"
      "s_cbranch_scc0 56f\n\t"
      "s_mov_b32 s89, 0\n\t"
      "55:\n\t"
      "s_add_u32 s80, s60, 3200\n\t"
      "buffer_load_dwordx4 v[0:3], v92, s[84:87], s60 offen offset:0 sc1\n\t"
      "buffer_load_dwordx4 v[4:7], v92, s[84:87], s60 offen offset:1024 sc1\n\t"
      "buffer_load_dwordx4 v[8:11], v92, s[84:87], s60 offen offset:2048 sc1\n\t"
      "buffer_load_dword v12, v93, s[84:87], s60 offen offset:3072 sc1\n\t"
      "buffer_load_dwordx4 v[16:19], v92, s[84:87], s80 offen offset:0 sc1\n\t"
      "buffer_load_dwordx4 v[20:23], v92, s[84:87], s80 offen offset:1024 sc1\n\t"
      "buffer_load_dwordx4 v[24:27], v92, s[84:87], s80 offen offset:2048 sc1\n\t"
      "buffer_load_dword v28, v93, s[84:87], s80 offen offset:3072 sc1\n\t"
      "s_bitset1_b32 s67, 0\n\t"
      "53:\n\t"
      "s_bitcmp1_b32 s67, 1\n\t"
      "s_cbranch_scc1 57f\n\t"
      "56:\n\t"
      "s_bitcmp1_b32 s67, 0\n\t"
      "s_cselect_b32 s78, s61, s60\n\t"
      "s_cselect_b32 s79, 4, 0\n\t"
      "s_lshr_b32 s79, s66, s79\n\t"
      "s_and_b32 s79, s79, 15\n\t"
      "s_cmp_eq_u32 s78, 22937600\n\t"
      "s_cbranch_scc1 57f\n\t"
      "s_cmp_gt_u32 s70, s79\n\t"
      "s_cbranch_scc1 57f\n\t"
      "s_add_u32 s72, s68, s78\n\t"
      "s_addc_u32 s73, s69, 0\n\t"
      "s_load_dwordx2 s[74:75], s[72:73], 3080 glc\n\t"
      "s_load_dwordx2 s[76:77], s[72:73], 6280 glc\n\t"
      "s_bitset1_b32 s67, 1\n\t"
      "57:\n\t"
      "s_cmp_eq_u32 s70, 0\n\t"
      "s_cbranch_scc1 58f\n\t"
      "s_cmp_lg_u32 s70, %[ld]\n\t"
      "s_cbranch_scc1 59f\n\t"
      "s_bitcmp1_b32 s67, 0\n\t"
      "s_cbranch_scc1 60f\n\t"
      "s_waitcnt vmcnt(0)\n\t"
      "60:\n\t"
      "s_waitcnt vmcnt(8)\n\t"
      "59:\n\t"
      "s_barrier\n\t"
      "s_sub_u32 s70, s70, 1\n\t"
      "s_branch 50b\n\t"
      "58:\n\t"
      "s_cmp_lg_u32 s60, 22937600\n\t"
      "s_cbranch_scc1 50b\n\t"
      "s_cmp_eq_u32 %[j0], 22937600\n\t"
      "s_cbranch_scc1 70f\n\t"
      "v_add_u32 v88, %[sums], v92\n\t"
      "v_mov_b32 v89, %[srdy]\n\t"
      "v_mov_b32 v90, 1\n\t"
      "s_nop 7\n\t"
      "ds_write_b128 v88, v[32:35] offset:0\n\t"
      "ds_write_b128 v88, v[36:39] offset:1024\n\t"
      "ds_write_b128 v88, v[40:43] offset:2048\n\t"
      "ds_write_b128 v88, v[44:47] offset:3072\n\t"
      "ds_write_b128 v88, v[48:51] offset:4096\n\t"
      "ds_write_b128 v88, v[52:55] offset:5120\n\t"
      "ds_write_b32 v89, v90\n\t"
      "ds_write_b128 v88, v[56:59] offset:6144\n\t"
      "ds_write_b128 v88, v[60:63] offset:7168\n\t"
      "ds_write_b128 v88, v[64:67] offset:8192\n\t"
      "ds_write_b128 v88, v[68:71] offset:9216\n\t"
      "ds_write_b128 v88, v[72:75] offset:10240\n\t"
      "ds_write_b128 v88, v[76:79] offset:11264\n\t"
      "ds_write_b32 v89, v90 offset:8\n\t"
      "70:\n\t"
      "s_waitcnt vmcnt(0) lgkmcnt(0)\n\t"
      "fa_endl:\n\t"
      :
      : [xsw] "s"(l_xsw), [wq] "s"(l_wq), [xp] "s"(l_xp), [xd] "s"(l_xd), [xdx] "s"(l_xdx), [wd] "s"(l_wd), [vp] "s"(l_vp), [kp] "s"(l_kp), [wot] "s"(l_wot), [pa] "s"(l_pa), [img] "s"(l_img), [qrdy] "s"(l_qrdy), [rot] "s"(l_rot), [xt] "s"(l_xt), [tsw] "s"(l_tsw), [part] "s"(r_part), [j0] "s"(r_j0), [j1] "s"(r_j1), [j2] "s"(r_j2), [j3] "s"(r_j3), [j4] "s"(r_j4), [due] "s"(r_due), [sums] "s"(r_sums), [srdy] "s"(r_srdy), [ld] "s"(r_ld), [g0] "s"(r_g0), [wo] "s"(r_wo), [od] "s"(r_od)
      : "v0", "v1", "v2", "v3", "v4", "v5", "v6", "v7", "v8", "v9", "v10", "v11", "v12", "v13", "v14", "v15", "v16", "v17", "v18", "v19", "v20", "v21", "v22", "v23", "v24", "v25", "v26", "v27", "v28", "v29", "v30", "v31", "v32", "v33", "v34", "v35", "v36", "v37", "v38", "v39", "v40", "v41", "v42", "v43", "v44", "v45", "v46", "v47", "v48", "v49", "v50", "v51", "v52", "v53", "v54", "v55", "v56", "v57", "v58", "v59", "v60", "v61", "v62", "v63", "v64", "v65", "v66", "v67", "v68", "v69", "v70", "v71", "v72", "v73", "v74", "v75", "v76", "v77", "v78", "v79", "v80", "v81", "v82", "v83", "v84", "v85", "v86", "v87", "v88", "v89", "v90", "v91", "v92", "v93", "v94", "v95", "v96", "v97", "v98", "v99", "v100", "v101", "v102", "v103", "v104", "v105", "v106", "v107", "v108", "v109", "v110", "v111", "v112", "v113", "v114", "v115", "v116", "v117", "v118", "v119", "v120", "v121", "v122", "v123", "v124", "v125", "v126", "v127", "v128", "v129", "v130", "v131", "v132", "v133", "v134", "v135", "v136", "v137", "v138", "v139", "v140", "v141", "v142", "v143", "v144", "v145", "v146", "v147", "v148", "v149", "v150", "v151", "v152", "v153", "v154", "v155", "v156", "v157", "v158", "v159", "v160", "v161", "v162", "v163", "v164", "v165", "v166", "v167", "v168", "v169", "v170", "v171", "v172", "v173", "v174", "v175", "v176", "v177", "v178", "v179", "v180", "v181", "v182", "v183", "v184", "v185", "v186", "v187", "v188", "v189", "v190", "v191", "v192", "v193", "v194", "v195", "v196", "v197", "v198", "v199", "v200", "v201", "v202", "v203", "v204", "v205", "v206", "v207", "v208", "v209", "v210", "v211", "v212", "v213", "v214", "v215", "v216", "v217", "v218", "v219", "v220", "v221", "v222", "v223", "v224", "v225", "v226", "v227", "v228", "v229", "v230", "v231", "v232", "v233", "v234", "v235", "v236", "v237", "v238", "v239", "v240", "v241", "v242", "v243", "v244", "v245", "v246", "v247", "v248", "v249", "v250", "v251", "v252", "v253", "v254", "v255", "s60", "s61", "s62", "s63", "s64", "s65", "s66", "s67", "s68", "s69", "s70", "s71", "s72", "s73", "s74", "s75", "s76", "s77", "s78", "s79", "s80", "s81", "s82", "s83", "s84", "s85", "s86", "s87", "s88", "s89", "s90", "s91", "s92", "s93", "s94", "s95", "s96", "s97", "scc", "vcc", "memory");
    return;
  }

  {
    const int w = wave, b = w & 1, hh = w >> 1;
    const u32 c_gw = g0 + 1, c_bsw = sw << 6;
    const char* c_wot = wog + (bp * 32 + w * 8) * 896;
    const u32 c_a1 = WRING + 256 * w, c_qimg = IMG + 2 * hh * IMG_U + 4 * b * 1024, c_qrdy = F_QRDY + 4 * w;
    const char* c_wop = wog + (size_t)(w * 32) * 896 + g0 * 128;
    const u32 c_pfd = ORING + w * 4 * O_PIECE, c_a2 = ORING + 64 * hh, c_oimg = IMG + b * IMG_U;
    const char* c_part = ws + WS_PART;
    const u32 c_pb = (u32)grp * 7 * GSTR + m * SSTR + w * WSTR;
    const u32 c_po = c_pb + mod7(g0 + 6) * GSTR;
    const int mo = m < 7 ? m : 0;
    const u32 ob = (u32)(grp * 7 + mo) * GSTR + w * WSTR;
    const u32 c_dl0 = ob + mod7(m + 1) * SSTR, c_dl1 = ob + 7 * SSTR;
    const u32 c_bump = m + w;
    const char* c_epoch = ws + WS_EPOCH + grp * 128;
    const u32 c_own = (14 - m) >> 3, c_sums = SUMS + w * 6144, c_srdy = F_SRDY + 8 * w;
    char* c_outp = a.out + (size_t)((2 * bp + b) * 48) * 1792 + (br * 448 + 64 * mo + 32 * hh) * 2;
    asm volatile(
      "v_mbcnt_lo_u32_b32 v255, -1, 0\n\t"
      "v_mbcnt_hi_u32_b32 v255, -1, v255\n\t"
      "s_waitcnt lgkmcnt(0)\n\t"
      "s_cmp_lg_u32 %[qrdy], 16\n\t"
      "s_cbranch_scc1 1f\n\t"
      "v_lshlrev_b32 v244, 2, v255\n\t"
      "v_mov_b32 v245, 0\n\t"
      "ds_write_b32 v244, v245\n\t"
      "global_load_dwordx2 v[246:247], v245, %[epoch] sc1\n\t"
      "s_waitcnt lgkmcnt(0)\n\t"
      "1:\n\t"
      "v_lshrrev_b32 v0, 4, v255\n\t"
      "v_bfe_u32 v1, v255, 2, 2\n\t"
      "v_lshl_add_u32 v1, v0, 3, v1\n\t"
      "v_mul_u32_u24 v1, 1056, v1\n\t"
      "v_lshl_add_u32 v1, v0, 7, v1\n\t"
      "v_and_b32 v2, 3, v255\n\t"
      "v_lshl_add_u32 v1, v2, 3, v1\n\t"
      "v_add_u32 v250, %[a1], v1\n\t"
      "v_add_u32 v248, 68608, v250\n\t"
      "v_xor_b32 v250, v248, v250\n\t"
      "v_and_b32 v1, 15, v255\n\t"
      "v_lshrrev_b32 v2, 1, v1\n\t"
      "v_xor_b32 v2, v0, v2\n\t"
      "v_lshlrev_b32 v2, 4, v2\n\t"
      "v_lshl_add_u32 v2, v1, 7, v2\n\t"
      "v_add_u32 v249, 137728, v2\n\t"
      "v_add_u32 v251, 12288, v249\n\t"
      "v_xor_b32 v251, v249, v251\n\t"
      "v_xor_b32 v251, 64, v251\n\t"
      "v_lshlrev_b32 v252, 4, v255\n\t"
      "v_add_u32 v252, %[qimg], v252\n\t"
      "v_xor_b32 v249, %[bsw], v249\n\t"
      "v_mov_b64 v[0:1], 0\n\t"
      "v_mov_b64 v[2:3], 0\n\t"
      "v_mov_b64 v[4:5], 0\n\t"
      "v_mov_b64 v[6:7], 0\n\t"
      "v_mov_b64 v[8:9], 0\n\t"
      "v_mov_b64 v[10:11], 0\n\t"
      "v_mov_b64 v[12:13], 0\n\t"
      "v_mov_b64 v[14:15], 0\n\t"
      "v_mov_b64 v[16:17], 0\n\t"
      "v_mov_b64 v[18:19], 0\n\t"
      "v_mov_b64 v[20:21], 0\n\t"
      "v_mov_b64 v[22:23], 0\n\t"
      "v_mov_b64 v[24:25], 0\n\t"
      "v_mov_b64 v[26:27], 0\n\t"
      "v_mov_b64 v[28:29], 0\n\t"
      "v_mov_b64 v[30:31], 0\n\t"
      "v_mov_b64 v[32:33], 0\n\t"
      "v_mov_b64 v[34:35], 0\n\t"
      "v_mov_b64 v[36:37], 0\n\t"
      "v_mov_b64 v[38:39], 0\n\t"
      "v_mov_b64 v[40:41], 0\n\t"
      "v_mov_b64 v[42:43], 0\n\t"
      "v_mov_b64 v[44:45], 0\n\t"
      "v_mov_b64 v[46:47], 0\n\t"
      "v_mov_b64 v[48:49], 0\n\t"
      "v_mov_b64 v[50:51], 0\n\t"
      "v_mov_b64 v[52:53], 0\n\t"
      "v_mov_b64 v[54:55], 0\n\t"
      "v_mov_b64 v[56:57], 0\n\t"
      "v_mov_b64 v[58:59], 0\n\t"
      "v_mov_b64 v[60:61], 0\n\t"
      "v_mov_b64 v[62:63], 0\n\t"
      "v_mov_b64 v[64:65], 0\n\t"
      "v_mov_b64 v[66:67], 0\n\t"
      "v_mov_b64 v[68:69], 0\n\t"
      "v_mov_b64 v[70:71], 0\n\t"
      "v_mov_b64 v[72:73], 0\n\t"
      "v_mov_b64 v[74:75], 0\n\t"
      "v_mov_b64 v[76:77], 0\n\t"
      "v_mov_b64 v[78:79], 0\n\t"
      "v_mov_b64 v[80:81], 0\n\t"
      "v_mov_b64 v[82:83], 0\n\t"
      "v_mov_b64 v[84:85], 0\n\t"
      "v_mov_b64 v[86:87], 0\n\t"
      "v_mov_b64 v[88:89], 0\n\t"
      "v_mov_b64 v[90:91], 0\n\t"
      "v_mov_b64 v[92:93], 0\n\t"
      "v_mov_b64 v[94:95], 0\n\t"
      "v_mov_b64 v[96:97], 0\n\t"
      "v_mov_b64 v[98:99], 0\n\t"
      "v_mov_b64 v[100:101], 0\n\t"
      "v_mov_b64 v[102:103], 0\n\t"
      "v_mov_b64 v[104:105], 0\n\t"
      "v_mov_b64 v[106:107], 0\n\t"
      "v_mov_b64 v[108:109], 0\n\t"
      "v_mov_b64 v[110:111], 0\n\t"
      "v_mov_b64 v[112:113], 0\n\t"
      "v_mov_b64 v[114:115], 0\n\t"
      "v_mov_b64 v[116:117], 0\n\t"
      "v_mov_b64 v[118:119], 0\n\t"
      "v_mov_b64 v[120:121], 0\n\t"
      "v_mov_b64 v[122:123], 0\n\t"
      "v_mov_b64 v[124:125], 0\n\t"
      "v_mov_b64 v[126:127], 0\n\t"
      "v_mov_b64 v[128:129], 0\n\t"
      "v_mov_b64 v[130:131], 0\n\t"
      "v_mov_b64 v[132:133], 0\n\t"
      "v_mov_b64 v[134:135], 0\n\t"
      "v_mov_b64 v[136:137], 0\n\t"
      "v_mov_b64 v[138:139], 0\n\t"
      "v_mov_b64 v[140:141], 0\n\t"
      "v_mov_b64 v[142:143], 0\n\t"
      "v_mov_b64 v[144:145], 0\n\t"
      "v_mov_b64 v[146:147], 0\n\t"
      "v_mov_b64 v[148:149], 0\n\t"
      "v_mov_b64 v[150:151], 0\n\t"
      "v_mov_b64 v[152:153], 0\n\t"
      "v_mov_b64 v[154:155], 0\n\t"
      "v_mov_b64 v[156:157], 0\n\t"
      "v_mov_b64 v[158:159], 0\n\t"
      "v_mov_b64 v[160:161], 0\n\t"
      "v_mov_b64 v[162:163], 0\n\t"
      "v_mov_b64 v[164:165], 0\n\t"
      "v_mov_b64 v[166:167], 0\n\t"
      "v_mov_b64 v[168:169], 0\n\t"
      "v_mov_b64 v[170:171], 0\n\t"
      "v_mov_b64 v[172:173], 0\n\t"
      "v_mov_b64 v[174:175], 0\n\t"
      "v_mov_b64 v[176:177], 0\n\t"
      "v_mov_b64 v[178:179], 0\n\t"
      "v_mov_b64 v[180:181], 0\n\t"
      "v_mov_b64 v[182:183], 0\n\t"
      "v_mov_b64 v[184:185], 0\n\t"
      "v_mov_b64 v[186:187], 0\n\t"
      "v_mov_b64 v[188:189], 0\n\t"
      "v_mov_b64 v[190:191], 0\n\t"
      "s_mov_b32 s88, 7\n\t"
      "s_barrier\n\t"
      "ds_read_b64_tr_b16 v[228:229], v248 offset:0\n\t"
      "ds_read_b64_tr_b16 v[230:231], v248 offset:4224\n\t"
      "ds_read_b128 v[192:195], v249 offset:0\n\t"
      "ds_read_b128 v[196:199], v249 offset:2048\n\t"
      "ds_read_b128 v[200:203], v249 offset:4096\n\t"
      "ds_read_b64_tr_b16 v[232:233], v248 offset:32\n\t"
      "ds_read_b64_tr_b16 v[234:235], v248 offset:4256\n\t"
      "ds_read_b128 v[224:227], v249 offset:10240\n\t"
      "ds_read_b128 v[220:223], v249 offset:8192\n\t"
      "ds_read_b128 v[216:219], v249 offset:6144\n\t"
      "ds_read_b64_tr_b16 v[236:237], v248 offset:64\n\t"
      "ds_read_b64_tr_b16 v[238:239], v248 offset:4288\n\t"
      "3:\n\t"
      "s_waitcnt lgkmcnt(9)\n\t"
      "v_mfma_f32_16x16x32_bf16 v[0:3], v[228:231], v[192:195], v[0:3]\n\t"
      "s_waitcnt lgkmcnt(8)\n\t"
      "v_mfma_f32_16x16x32_bf16 v[4:7], v[228:231], v[196:199], v[4:7]\n\t"
      "s_waitcnt lgkmcnt(7)\n\t"
      "v_mfma_f32_16x16x32_bf16 v[8:11], v[228:231], v[200:203], v[8:11]\n\t"
      "ds_read_b64_tr_b16 v[240:241], v248 offset:96\n\t"
      "s_waitcnt lgkmcnt(6)\n\t"
      "v_mfma_f32_16x16x32_bf16 v[32:35], v[232:235], v[200:203], v[32:35]\n\t"
      "ds_read_b64_tr_b16 v[242:243], v248 offset:4320\n\t"
      "v_mfma_f32_16x16x32_bf16 v[28:31], v[232:235], v[196:199], v[28:31]\n\t"
      "v_mfma_f32_16x16x32_bf16 v[24:27], v[232:235], v[192:195], v[24:27]\n\t"
      "s_waitcnt lgkmcnt(4)\n\t"
      "v_mfma_f32_16x16x32_bf16 v[12:15], v[228:231], v[216:219], v[12:15]\n\t"
      "v_mfma_f32_16x16x32_bf16 v[16:19], v[228:231], v[220:223], v[16:19]\n\t"
      "v_mfma_f32_16x16x32_bf16 v[20:23], v[228:231], v[224:227], v[20:23]\n\t"
      "v_mfma_f32_16x16x32_bf16 v[44:47], v[232:235], v[224:227], v[44:47]\n\t"
      "ds_read_b64_tr_b16 v[228:229], v248 offset:128\n\t"
      "v_mfma_f32_16x16x32_bf16 v[40:43], v[232:235], v[220:223], v[40:43]\n\t"
      "ds_read_b64_tr_b16 v[230:231], v248 offset:4352\n\t"
      "v_mfma_f32_16x16x32_bf16 v[36:39], v[232:235], v[216:219], v[36:39]\n\t"
      "s_waitcnt lgkmcnt(4)\n\t"
      "v_mfma_f32_16x16x32_bf16 v[68:71], v[236:239], v[224:227], v[68:71]\n\t"
      "v_mfma_f32_16x16x32_bf16 v[64:67], v[236:239], v[220:223], v[64:67]\n\t"
      "ds_read_b64_tr_b16 v[232:233], v248 offset:160\n\t"
      "v_mfma_f32_16x16x32_bf16 v[60:63], v[236:239], v[216:219], v[60:63]\n\t"
      "ds_read_b64_tr_b16 v[234:235], v248 offset:4384\n\t"
      "v_mfma_f32_16x16x32_bf16 v[56:59], v[236:239], v[200:203], v[56:59]\n\t"
      "v_mfma_f32_16x16x32_bf16 v[52:55], v[236:239], v[196:199], v[52:55]\n\t"
      "v_mfma_f32_16x16x32_bf16 v[48:51], v[236:239], v[192:195], v[48:51]\n\t"
      "s_waitcnt lgkmcnt(4)\n\t"
      "v_mfma_f32_16x16x32_bf16 v[72:75], v[240:243], v[192:195], v[72:75]\n\t"
      "v_mfma_f32_16x16x32_bf16 v[76:79], v[240:243], v[196:199], v[76:79]\n\t"
      "ds_read_b64_tr_b16 v[236:237], v248 offset:192\n\t"
      "v_mfma_f32_16x16x32_bf16 v[80:83], v[240:243], v[200:203], v[80:83]\n\t"
      "ds_read_b64_tr_b16 v[238:239], v248 offset:4416\n\t"
      "v_mfma_f32_16x16x32_bf16 v[84:87], v[240:243], v[216:219], v[84:87]\n\t"
      "v_mfma_f32_16x16x32_bf16 v[88:91], v[240:243], v[220:223], v[88:91]\n\t"
      "v_mfma_f32_16x16x32_bf16 v[92:95], v[240:243], v[224:227], v[92:95]\n\t"
      "s_waitcnt lgkmcnt(4)\n\t"
      "v_mfma_f32_16x16x32_bf16 v[116:119], v[228:231], v[224:227], v[116:119]\n\t"
      "ds_read_b64_tr_b16 v[240:241], v248 offset:224\n\t"
      "v_mfma_f32_16x16x32_bf16 v[112:115], v[228:231], v[220:223], v[112:115]\n\t"
      "ds_read_b64_tr_b16 v[242:243], v248 offset:4448\n\t"
      "v_mfma_f32_16x16x32_bf16 v[108:111], v[228:231], v[216:219], v[108:111]\n\t"
      "v_mfma_f32_16x16x32_bf16 v[104:107], v[228:231], v[200:203], v[104:107]\n\t"
      "v_xor_b32 v249, 64, v249\n\t"
      "v_mfma_f32_16x16x32_bf16 v[100:103], v[228:231], v[196:199], v[100:103]\n\t"
      "v_mfma_f32_16x16x32_bf16 v[96:99], v[228:231], v[192:195], v[96:99]\n\t"
      "s_waitcnt lgkmcnt(4)\n\t"
      "v_mfma_f32_16x16x32_bf16 v[120:123], v[232:235], v[192:195], v[120:123]\n\t"
      "s_barrier\n\t"
      "v_mfma_f32_16x16x32_bf16 v[124:127], v[232:235], v[196:199], v[124:127]\n\t"
      "ds_read_b64_tr_b16 v[228:229], v248 offset:34304\n\t"
      "v_mfma_f32_16x16x32_bf16 v[128:131], v[232:235], v[200:203], v[128:131]\n\t"
      "ds_read_b64_tr_b16 v[230:231], v248 offset:38528\n\t"
      "v_mfma_f32_16x16x32_bf16 v[132:135], v[232:235], v[216:219], v[132:135]\n\t"
      "ds_read_b128 v[204:207], v249 offset:0\n\t"
      "v_mfma_f32_16x16x32_bf16 v[136:139], v[232:235], v[220:223], v[136:139]\n\t"
      "ds_read_b128 v[208:211], v249 offset:2048\n\t"
      "v_mfma_f32_16x16x32_bf16 v[140:143], v[232:235], v[224:227], v[140:143]\n\t"
      "ds_read_b128 v[212:215], v249 offset:4096\n\t"
      "s_waitcnt lgkmcnt(7)\n\t"
      "v_mfma_f32_16x16x32_bf16 v[156:159], v[236:239], v[216:219], v[156:159]\n\t"
      "ds_read_b64_tr_b16 v[232:233], v248 offset:34336\n\t"
      "v_mfma_f32_16x16x32_bf16 v[160:163], v[236:239], v[220:223], v[160:163]\n\t"
      "ds_read_b64_tr_b16 v[234:235], v248 offset:38560\n\t"
      "v_mfma_f32_16x16x32_bf16 v[164:167], v[236:239], v[224:227], v[164:167]\n\t"
      "s_waitcnt lgkmcnt(7)\n\t"
      "v_mfma_f32_16x16x32_bf16 v[188:191], v[240:243], v[224:227], v[188:191]\n\t"
      "ds_read_b128 v[224:227], v249 offset:10240\n\t"
      "v_mfma_f32_16x16x32_bf16 v[184:187], v[240:243], v[220:223], v[184:187]\n\t"
      "ds_read_b128 v[220:223], v249 offset:8192\n\t"
      "v_mfma_f32_16x16x32_bf16 v[180:183], v[240:243], v[216:219], v[180:183]\n\t"
      "ds_read_b128 v[216:219], v249 offset:6144\n\t"
      "v_mfma_f32_16x16x32_bf16 v[144:147], v[236:239], v[192:195], v[144:147]\n\t"
      "s_sub_u32 s88, s88, 1\n\t"
      "v_mfma_f32_16x16x32_bf16 v[148:151], v[236:239], v[196:199], v[148:151]\n\t"
      "s_cmp_eq_u32 s88, 0\n\t"
      "v_mfma_f32_16x16x32_bf16 v[152:155], v[236:239], v[200:203], v[152:155]\n\t"
      "v_mfma_f32_16x16x32_bf16 v[176:179], v[240:243], v[200:203], v[176:179]\n\t"
      "ds_read_b64_tr_b16 v[236:237], v248 offset:34368\n\t"
      "v_mfma_f32_16x16x32_bf16 v[172:175], v[240:243], v[196:199], v[172:175]\n\t"
      "ds_read_b64_tr_b16 v[238:239], v248 offset:38592\n\t"
      "v_mfma_f32_16x16x32_bf16 v[168:171], v[240:243], v[192:195], v[168:171]\n\t"
      "s_cbranch_scc1 4f\n\t"
      "s_waitcnt lgkmcnt(9)\n\t"
      "v_mfma_f32_16x16x32_bf16 v[0:3], v[228:231], v[204:207], v[0:3]\n\t"
      "s_waitcnt lgkmcnt(8)\n\t"
      "v_mfma_f32_16x16x32_bf16 v[4:7], v[228:231], v[208:211], v[4:7]\n\t"
      "s_waitcnt lgkmcnt(7)\n\t"
      "v_mfma_f32_16x16x32_bf16 v[8:11], v[228:231], v[212:215], v[8:11]\n\t"
      "ds_read_b64_tr_b16 v[240:241], v248 offset:34400\n\t"
      "s_waitcnt lgkmcnt(6)\n\t"
      "v_mfma_f32_16x16x32_bf16 v[32:35], v[232:235], v[212:215], v[32:35]\n\t"
      "ds_read_b64_tr_b16 v[242:243], v248 offset:38624\n\t"
      "v_mfma_f32_16x16x32_bf16 v[28:31], v[232:235], v[208:211], v[28:31]\n\t"
      "v_mfma_f32_16x16x32_bf16 v[24:27], v[232:235], v[204:207], v[24:27]\n\t"
      "s_waitcnt lgkmcnt(4)\n\t"
      "v_mfma_f32_16x16x32_bf16 v[12:15], v[228:231], v[216:219], v[12:15]\n\t"
      "v_mfma_f32_16x16x32_bf16 v[16:19], v[228:231], v[220:223], v[16:19]\n\t"
      "v_mfma_f32_16x16x32_bf16 v[20:23], v[228:231], v[224:227], v[20:23]\n\t"
      "v_mfma_f32_16x16x32_bf16 v[44:47], v[232:235], v[224:227], v[44:47]\n\t"
      "ds_read_b64_tr_b16 v[228:229], v248 offset:34432\n\t"
      "v_mfma_f32_16x16x32_bf16 v[40:43], v[232:235], v[220:223], v[40:43]\n\t"
      "ds_read_b64_tr_b16 v[230:231], v248 offset:38656\n\t"
      "v_mfma_f32_16x16x32_bf16 v[36:39], v[232:235], v[216:219], v[36:39]\n\t"
      "s_waitcnt lgkmcnt(4)\n\t"
      "v_mfma_f32_16x16x32_bf16 v[68:71], v[236:239], v[224:227], v[68:71]\n\t"
      "v_mfma_f32_16x16x32_bf16 v[64:67], v[236:239], v[220:223], v[64:67]\n\t"
      "ds_read_b64_tr_b16 v[232:233], v248 offset:34464\n\t"
      "v_mfma_f32_16x16x32_bf16 v[60:63], v[236:239], v[216:219], v[60:63]\n\t"
      "ds_read_b64_tr_b16 v[234:235], v248 offset:38688\n\t"
      "v_mfma_f32_16x16x32_bf16 v[56:59], v[236:239], v[212:215], v[56:59]\n\t"
      "v_mfma_f32_16x16x32_bf16 v[52:55], v[236:239], v[208:211], v[52:55]\n\t"
      "v_mfma_f32_16x16x32_bf16 v[48:51], v[236:239], v[204:207], v[48:51]\n\t"
      "s_waitcnt lgkmcnt(4)\n\t"
      "v_mfma_f32_16x16x32_bf16 v[72:75], v[240:243], v[204:207], v[72:75]\n\t"
      "v_mfma_f32_16x16x32_bf16 v[76:79], v[240:243], v[208:211], v[76:79]\n\t"
      "ds_read_b64_tr_b16 v[236:237], v248 offset:34496\n\t"
      "v_mfma_f32_16x16x32_bf16 v[80:83], v[240:243], v[212:215], v[80:83]\n\t"
      "ds_read_b64_tr_b16 v[238:239], v248 offset:38720\n\t"
      "v_mfma_f32_16x16x32_bf16 v[84:87], v[240:243], v[216:219], v[84:87]\n\t"
      "v_mfma_f32_16x16x32_bf16 v[88:91], v[240:243], v[220:223], v[88:91]\n\t"
      "v_mfma_f32_16x16x32_bf16 v[92:95], v[240:243], v[224:227], v[92:95]\n\t"
      "s_waitcnt lgkmcnt(4)\n\t"
      "v_mfma_f32_16x16x32_bf16 v[116:119], v[228:231], v[224:227], v[116:119]\n\t"
      "ds_read_b64_tr_b16 v[240:241], v248 offset:34528\n\t"
      "v_mfma_f32_16x16x32_bf16 v[112:115], v[228:231], v[220:223], v[112:115]\n\t"
      "ds_read_b64_tr_b16 v[242:243], v248 offset:38752\n\t"
      "v_mfma_f32_16x16x32_bf16 v[108:111], v[228:231], v[216:219], v[108:111]\n\t"
      "v_mfma_f32_16x16x32_bf16 v[104:107], v[228:231], v[212:215], v[104:107]\n\t"
      "v_xor_b32 v249, v251, v249\n\t"
      "v_mfma_f32_16x16x32_bf16 v[100:103], v[228:231], v[208:211], v[100:103]\n\t"
      "v_xor_b32 v248, v250, v248\n\t"
      "v_mfma_f32_16x16x32_bf16 v[96:99], v[228:231], v[204:207], v[96:99]\n\t"
      "s_waitcnt lgkmcnt(4)\n\t"
      "v_mfma_f32_16x16x32_bf16 v[120:123], v[232:235], v[204:207], v[120:123]\n\t"
      "s_barrier\n\t"
      "v_mfma_f32_16x16x32_bf16 v[124:127], v[232:235], v[208:211], v[124:127]\n\t"
      "ds_read_b64_tr_b16 v[228:229], v248 offset:0\n\t"
      "v_mfma_f32_16x16x32_bf16 v[128:131], v[232:235], v[212:215], v[128:131]\n\t"
      "ds_read_b64_tr_b16 v[230:231], v248 offset:4224\n\t"
      "v_mfma_f32_16x16x32_bf16 v[132:135], v[232:235], v[216:219], v[132:135]\n\t"
      "ds_read_b128 v[192:195], v249 offset:0\n\t"
      "v_mfma_f32_16x16x32_bf16 v[136:139], v[232:235], v[220:223], v[136:139]\n\t"
      "ds_read_b128 v[196:199], v249 offset:2048\n\t"
      "v_mfma_f32_16x16x32_bf16 v[140:143], v[232:235], v[224:227], v[140:143]\n\t"
      "ds_read_b128 v[200:203], v249 offset:4096\n\t"
      "s_waitcnt lgkmcnt(7)\n\t"
      "v_mfma_f32_16x16x32_bf16 v[156:159], v[236:239], v[216:219], v[156:159]\n\t"
      "ds_read_b64_tr_b16 v[232:233], v248 offset:32\n\t"
      "v_mfma_f32_16x16x32_bf16 v[160:163], v[236:239], v[220:223], v[160:163]\n\t"
      "ds_read_b64_tr_b16 v[234:235], v248 offset:4256\n\t"
      "v_mfma_f32_16x16x32_bf16 v[164:167], v[236:239], v[224:227], v[164:167]\n\t"
      "s_waitcnt lgkmcnt(7)\n\t"
      "v_mfma_f32_16x16x32_bf16 v[188:191], v[240:243], v[224:227], v[188:191]\n\t"
      "ds_read_b128 v[224:227], v249 offset:10240\n\t"
      "v_mfma_f32_16x16x32_bf16 v[184:187], v[240:243], v[220:223], v[184:187]\n\t"
      "ds_read_b128 v[220:223], v249 offset:8192\n\t"
      "v_mfma_f32_16x16x32_bf16 v[180:183], v[240:243], v[216:219], v[180:183]\n\t"
      "ds_read_b128 v[216:219], v249 offset:6144\n\t"
      "v_mfma_f32_16x16x32_bf16 v[144:147], v[236:239], v[204:207], v[144:147]\n\t"
      "v_mfma_f32_16x16x32_bf16 v[148:151], v[236:239], v[208:211], v[148:151]\n\t"
      "v_mfma_f32_16x16x32_bf16 v[152:155], v[236:239], v[212:215], v[152:155]\n\t"
      "v_mfma_f32_16x16x32_bf16 v[176:179], v[240:243], v[212:215], v[176:179]\n\t"
      "ds_read_b64_tr_b16 v[236:237], v248 offset:64\n\t"
      "v_mfma_f32_16x16x32_bf16 v[172:175], v[240:243], v[208:211], v[172:175]\n\t"
      "ds_read_b64_tr_b16 v[238:239], v248 offset:4288\n\t"
      "v_mfma_f32_16x16x32_bf16 v[168:171], v[240:243], v[204:207], v[168:171]\n\t"
      "s_branch 3b\n\t"
      "4:\n\t"
      "s_waitcnt vmcnt(0)\n\t"
      "v_mov_b32 v253, %[qrdy]\n\t"
      "s_cmp_lg_u32 %[qrdy], 16\n\t"
      "s_cbranch_scc1 1f\n\t"
      "v_add_co_u32 v246, vcc, 0x7f4a7c15, v246\n\t"
      "v_mov_b32 v254, 0x9e3779b9\n\t"
      "s_nop 0\n\t"
      "v_addc_co_u32 v247, vcc, v247, v254, vcc\n\t"
      "ds_write_b64 v253, v[246:247] offset:64\n\t"
      "1:\n\t"
      "s_waitcnt lgkmcnt(9)\n\t"
      "v_mfma_f32_16x16x32_bf16 v[0:3], v[228:231], v[204:207], v[0:3]\n\t"
      "ds_read_b64_tr_b16 v[240:241], v248 offset:34400\n\t"
      "s_waitcnt lgkmcnt(6)\n\t"
      "v_mfma_f32_16x16x32_bf16 v[24:27], v[232:235], v[204:207], v[24:27]\n\t"
      "ds_read_b64_tr_b16 v[242:243], v248 offset:38624\n\t"
      "s_barrier\n\t"
      "v_mfma_f32_16x16x32_bf16 v[4:7], v[228:231], v[208:211], v[4:7]\n\t"
      "ds_read_b64_tr_b16 v[192:193], v248 offset:34432\n\t"
      "v_mfma_f32_16x16x32_bf16 v[28:31], v[232:235], v[208:211], v[28:31]\n\t"
      "ds_read_b64_tr_b16 v[194:195], v248 offset:38656\n\t"
      "v_cvt_pk_bf16_f32 v0, v0, v1\n\t"
      "v_cvt_pk_bf16_f32 v1, v2, v3\n\t"
      "s_waitcnt lgkmcnt(6)\n\t"
      "v_mfma_f32_16x16x32_bf16 v[12:15], v[228:231], v[216:219], v[12:15]\n\t"
      "ds_read_b64_tr_b16 v[196:197], v248 offset:34464\n\t"
      "v_cvt_pk_bf16_f32 v2, v24, v25\n\t"
      "v_cvt_pk_bf16_f32 v3, v26, v27\n\t"
      "v_mfma_f32_16x16x32_bf16 v[36:39], v[232:235], v[216:219], v[36:39]\n\t"
      "ds_read_b64_tr_b16 v[198:199], v248 offset:38688\n\t"
      "ds_write_b128 v252, v[0:3] offset:0\n\t"
      "v_mfma_f32_16x16x32_bf16 v[16:19], v[228:231], v[220:223], v[16:19]\n\t"
      "ds_read_b64_tr_b16 v[200:201], v248 offset:34496\n\t"
      "v_mfma_f32_16x16x32_bf16 v[40:43], v[232:235], v[220:223], v[40:43]\n\t"
      "ds_read_b64_tr_b16 v[202:203], v248 offset:38720\n\t"
      "v_cvt_pk_bf16_f32 v12, v12, v13\n\t"
      "v_cvt_pk_bf16_f32 v13, v14, v15\n\t"
      "s_waitcnt lgkmcnt(9)\n\t"
      "v_mfma_f32_16x16x32_bf16 v[48:51], v[236:239], v[204:207], v[48:51]\n\t"
      "ds_read_b64_tr_b16 v[244:245], v248 offset:34528\n\t"
      "v_cvt_pk_bf16_f32 v14, v36, v37\n\t"
      "v_cvt_pk_bf16_f32 v15, v38, v39\n\t"
      "v_mfma_f32_16x16x32_bf16 v[60:63], v[236:239], v[216:219], v[60:63]\n\t"
      "ds_read_b64_tr_b16 v[246:247], v248 offset:38752\n\t"
      "ds_write_b128 v252, v[12:15] offset:24576\n\t"
      "s_waitcnt lgkmcnt(10)\n\t"
      "v_mfma_f32_16x16x32_bf16 v[72:75], v[240:243], v[204:207], v[72:75]\n\t"
      "v_cvt_pk_bf16_f32 v4, v4, v5\n\t"
      "v_cvt_pk_bf16_f32 v5, v6, v7\n\t"
      "v_mfma_f32_16x16x32_bf16 v[84:87], v[240:243], v[216:219], v[84:87]\n\t"
      "v_cvt_pk_bf16_f32 v6, v28, v29\n\t"
      "v_cvt_pk_bf16_f32 v7, v30, v31\n\t"
      "s_waitcnt lgkmcnt(8)\n\t"
      "v_mfma_f32_16x16x32_bf16 v[96:99], v[192:195], v[204:207], v[96:99]\n\t"
      "ds_write_b128 v252, v[4:7] offset:8192\n\t"
      "v_mfma_f32_16x16x32_bf16 v[108:111], v[192:195], v[216:219], v[108:111]\n\t"
      "v_cvt_pk_bf16_f32 v48, v48, v49\n\t"
      "v_cvt_pk_bf16_f32 v49, v50, v51\n\t"
      "s_waitcnt lgkmcnt(7)\n\t"
      "v_mfma_f32_16x16x32_bf16 v[120:123], v[196:199], v[204:207], v[120:123]\n\t"
      "v_cvt_pk_bf16_f32 v50, v72, v73\n\t"
      "v_cvt_pk_bf16_f32 v51, v74, v75\n\t"
      "v_mfma_f32_16x16x32_bf16 v[132:135], v[196:199], v[216:219], v[132:135]\n\t"
      "ds_write_b128 v252, v[48:51] offset:1024\n\t"
      "s_waitcnt lgkmcnt(5)\n\t"
      "v_mfma_f32_16x16x32_bf16 v[144:147], v[200:203], v[204:207], v[144:147]\n\t"
      "v_cvt_pk_bf16_f32 v60, v60, v61\n\t"
      "v_cvt_pk_bf16_f32 v61, v62, v63\n\t"
      "v_mfma_f32_16x16x32_bf16 v[156:159], v[200:203], v[216:219], v[156:159]\n\t"
      "v_cvt_pk_bf16_f32 v62, v84, v85\n\t"
      "v_cvt_pk_bf16_f32 v63, v86, v87\n\t"
      "s_waitcnt lgkmcnt(3)\n\t"
      "v_mfma_f32_16x16x32_bf16 v[168:171], v[244:247], v[204:207], v[168:171]\n\t"
      "ds_write_b128 v252, v[60:63] offset:25600\n\t"
      "v_mfma_f32_16x16x32_bf16 v[180:183], v[244:247], v[216:219], v[180:183]\n\t"
      "v_cvt_pk_bf16_f32 v96, v96, v97\n\t"
      "v_cvt_pk_bf16_f32 v97, v98, v99\n\t"
      "v_mfma_f32_16x16x32_bf16 v[52:55], v[236:239], v[208:211], v[52:55]\n\t"
      "v_cvt_pk_bf16_f32 v98, v120, v121\n\t"
      "v_cvt_pk_bf16_f32 v99, v122, v123\n\t"
      "v_mfma_f32_16x16x32_bf16 v[64:67], v[236:239], v[220:223], v[64:67]\n\t"
      "ds_write_b128 v252, v[96:99] offset:2048\n\t"
      "v_mfma_f32_16x16x32_bf16 v[76:79], v[240:243], v[208:211], v[76:79]\n\t"
      "v_cvt_pk_bf16_f32 v108, v108, v109\n\t"
      "v_cvt_pk_bf16_f32 v109, v110, v111\n\t"
      "v_mfma_f32_16x16x32_bf16 v[88:91], v[240:243], v[220:223], v[88:91]\n\t"
      "v_cvt_pk_bf16_f32 v110, v132, v133\n\t"
      "v_cvt_pk_bf16_f32 v111, v134, v135\n\t"
      "v_mfma_f32_16x16x32_bf16 v[100:103], v[192:195], v[208:211], v[100:103]\n\t"
      "ds_write_b128 v252, v[108:111] offset:26624\n\t"
      "v_mfma_f32_16x16x32_bf16 v[112:115], v[192:195], v[220:223], v[112:115]\n\t"
      "v_cvt_pk_bf16_f32 v144, v144, v145\n\t"
      "v_cvt_pk_bf16_f32 v145, v146, v147\n\t"
      "v_mfma_f32_16x16x32_bf16 v[124:127], v[196:199], v[208:211], v[124:127]\n\t"
      "v_cvt_pk_bf16_f32 v146, v168, v169\n\t"
      "v_cvt_pk_bf16_f32 v147, v170, v171\n\t"
      "v_mfma_f32_16x16x32_bf16 v[136:139], v[196:199], v[220:223], v[136:139]\n\t"
      "ds_write_b128 v252, v[144:147] offset:3072\n\t"
      "v_mfma_f32_16x16x32_bf16 v[148:151], v[200:203], v[208:211], v[148:151]\n\t"
      "v_cvt_pk_bf16_f32 v156, v156, v157\n\t"
      "v_cvt_pk_bf16_f32 v157, v158, v159\n\t"
      "v_mfma_f32_16x16x32_bf16 v[160:163], v[200:203], v[220:223], v[160:163]\n\t"
      "v_cvt_pk_bf16_f32 v158, v180, v181\n\t"
      "v_cvt_pk_bf16_f32 v159, v182, v183\n\t"
      "v_mfma_f32_16x16x32_bf16 v[172:175], v[244:247], v[208:211], v[172:175]\n\t"
      "ds_write_b128 v252, v[156:159] offset:27648\n\t"
      "v_mfma_f32_16x16x32_bf16 v[184:187], v[244:247], v[220:223], v[184:187]\n\t"
      "v_cvt_pk_bf16_f32 v16, v16, v17\n\t"
      "v_cvt_pk_bf16_f32 v17, v18, v19\n\t"
      "v_mfma_f32_16x16x32_bf16 v[8:11], v[228:231], v[212:215], v[8:11]\n\t"
      "v_cvt_pk_bf16_f32 v18, v40, v41\n\t"
      "v_cvt_pk_bf16_f32 v19, v42, v43\n\t"
      "v_mfma_f32_16x16x32_bf16 v[20:23], v[228:231], v[224:227], v[20:23]\n\t"
      "ds_write_b128 v252, v[16:19] offset:32768\n\t"
      "v_mfma_f32_16x16x32_bf16 v[32:35], v[232:235], v[212:215], v[32:35]\n\t"
      "s_waitcnt lgkmcnt(1)\n\t"
      "s_barrier\n\t"
      "v_cvt_pk_bf16_f32 v52, v52, v53\n\t"
      "v_cvt_pk_bf16_f32 v53, v54, v55\n\t"
      "v_mfma_f32_16x16x32_bf16 v[44:47], v[232:235], v[224:227], v[44:47]\n\t"
      "v_cvt_pk_bf16_f32 v54, v76, v77\n\t"
      "v_cvt_pk_bf16_f32 v55, v78, v79\n\t"
      "v_mfma_f32_16x16x32_bf16 v[56:59], v[236:239], v[212:215], v[56:59]\n\t"
      "ds_write_b128 v252, v[52:55] offset:9216\n\t"
      "v_mfma_f32_16x16x32_bf16 v[68:71], v[236:239], v[224:227], v[68:71]\n\t"
      "v_cvt_pk_bf16_f32 v64, v64, v65\n\t"
      "v_cvt_pk_bf16_f32 v65, v66, v67\n\t"
      "v_mfma_f32_16x16x32_bf16 v[80:83], v[240:243], v[212:215], v[80:83]\n\t"
      "v_cvt_pk_bf16_f32 v66, v88, v89\n\t"
      "v_cvt_pk_bf16_f32 v67, v90, v91\n\t"
      "v_mfma_f32_16x16x32_bf16 v[92:95], v[240:243], v[224:227], v[92:95]\n\t"
      "ds_write_b128 v252, v[64:67] offset:33792\n\t"
      "v_mfma_f32_16x16x32_bf16 v[104:107], v[192:195], v[212:215], v[104:107]\n\t"
      "v_cvt_pk_bf16_f32 v100, v100, v101\n\t"
      "v_cvt_pk_bf16_f32 v101, v102, v103\n\t"
      "v_mfma_f32_16x16x32_bf16 v[116:119], v[192:195], v[224:227], v[116:119]\n\t"
      "v_cvt_pk_bf16_f32 v102, v124, v125\n\t"
      "v_cvt_pk_bf16_f32 v103, v126, v127\n\t"
      "v_mfma_f32_16x16x32_bf16 v[128:131], v[196:199], v[212:215], v[128:131]\n\t"
      "ds_write_b128 v252, v[100:103] offset:10240\n\t"
      "v_mfma_f32_16x16x32_bf16 v[140:143], v[196:199], v[224:227], v[140:143]\n\t"
      "v_cvt_pk_bf16_f32 v112, v112, v113\n\t"
      "v_cvt_pk_bf16_f32 v113, v114, v115\n\t"
      "v_mfma_f32_16x16x32_bf16 v[152:155], v[200:203], v[212:215], v[152:155]\n\t"
      "v_cvt_pk_bf16_f32 v114, v136, v137\n\t"
      "v_cvt_pk_bf16_f32 v115, v138, v139\n\t"
      "v_mfma_f32_16x16x32_bf16 v[164:167], v[200:203], v[224:227], v[164:167]\n\t"
      "ds_write_b128 v252, v[112:115] offset:34816\n\t"
      "v_mfma_f32_16x16x32_bf16 v[176:179], v[244:247], v[212:215], v[176:179]\n\t"
      "v_cvt_pk_bf16_f32 v148, v148, v149\n\t"
      "v_cvt_pk_bf16_f32 v149, v150, v151\n\t"
      "v_mfma_f32_16x16x32_bf16 v[188:191], v[244:247], v[224:227], v[188:191]\n\t"
      "v_cvt_pk_bf16_f32 v150, v172, v173\n\t"
      "v_cvt_pk_bf16_f32 v151, v174, v175\n\t"
      "ds_write_b128 v252, v[148:151] offset:11264\n\t"
      "v_cvt_pk_bf16_f32 v160, v160, v161\n\t"
      "v_cvt_pk_bf16_f32 v161, v162, v163\n\t"
      "v_cvt_pk_bf16_f32 v162, v184, v185\n\t"
      "v_cvt_pk_bf16_f32 v163, v186, v187\n\t"
      "ds_write_b128 v252, v[160:163] offset:35840\n\t"
      "v_mov_b32 v254, 2\n\t"
      "ds_write_b32 v253, v254\n\t"
      "v_cvt_pk_bf16_f32 v8, v8, v9\n\t"
      "v_cvt_pk_bf16_f32 v9, v10, v11\n\t"
      "v_cvt_pk_bf16_f32 v10, v32, v33\n\t"
      "v_cvt_pk_bf16_f32 v11, v34, v35\n\t"
      "ds_write_b128 v252, v[8:11] offset:16384\n\t"
      "v_cvt_pk_bf16_f32 v20, v20, v21\n\t"
      "v_cvt_pk_bf16_f32 v21, v22, v23\n\t"
      "v_cvt_pk_bf16_f32 v22, v44, v45\n\t"
      "v_cvt_pk_bf16_f32 v23, v46, v47\n\t"
      "ds_write_b128 v252, v[20:23] offset:40960\n\t"
      "v_cvt_pk_bf16_f32 v56, v56, v57\n\t"
      "v_cvt_pk_bf16_f32 v57, v58, v59\n\t"
      "v_cvt_pk_bf16_f32 v58, v80, v81\n\t"
      "v_cvt_pk_bf16_f32 v59, v82, v83\n\t"
      "ds_write_b128 v252, v[56:59] offset:17408\n\t"
      "v_cvt_pk_bf16_f32 v68, v68, v69\n\t"
      "v_cvt_pk_bf16_f32 v69, v70, v71\n\t"
      "v_cvt_pk_bf16_f32 v70, v92, v93\n\t"
      "v_cvt_pk_bf16_f32 v71, v94, v95\n\t"
      "ds_write_b128 v252, v[68:71] offset:41984\n\t"
      "v_cvt_pk_bf16_f32 v104, v104, v105\n\t"
      "v_cvt_pk_bf16_f32 v105, v106, v107\n\t"
      "v_cvt_pk_bf16_f32 v106, v128, v129\n\t"
      "v_cvt_pk_bf16_f32 v107, v130, v131\n\t"
      "ds_write_b128 v252, v[104:107] offset:18432\n\t"
      "v_cvt_pk_bf16_f32 v116, v116, v117\n\t"
      "v_cvt_pk_bf16_f32 v117, v118, v119\n\t"
      "v_cvt_pk_bf16_f32 v118, v140, v141\n\t"
      "v_cvt_pk_bf16_f32 v119, v142, v143\n\t"
      "ds_write_b128 v252, v[116:119] offset:43008\n\t"
      "v_cvt_pk_bf16_f32 v152, v152, v153\n\t"
      "v_cvt_pk_bf16_f32 v153, v154, v155\n\t"
      "v_cvt_pk_bf16_f32 v154, v176, v177\n\t"
      "v_cvt_pk_bf16_f32 v155, v178, v179\n\t"
      "ds_write_b128 v252, v[152:155] offset:19456\n\t"
      "v_cvt_pk_bf16_f32 v164, v164, v165\n\t"
      "v_cvt_pk_bf16_f32 v165, v166, v167\n\t"
      "v_cvt_pk_bf16_f32 v166, v188, v189\n\t"
      "v_cvt_pk_bf16_f32 v167, v190, v191\n\t"
      "ds_write_b128 v252, v[164:167] offset:44032\n\t"
      "v_mov_b32 v254, 3\n\t"
      "ds_write_b32 v253, v254\n\t"
      "v_mbcnt_lo_u32_b32 v255, -1, 0\n\t"
      "v_mbcnt_hi_u32_b32 v255, -1, v255\n\t"
      "v_mov_b32 v249, 16\n\t"
      "v_lshrrev_b32 v240, 3, v255\n\t"
      "v_mul_u32_u24 v240, 896, v240\n\t"
      "v_and_b32 v241, 7, v255\n\t"
      "v_lshl_add_u32 v240, v241, 4, v240\n\t"
      "v_add_u32 v241, 7168, v240\n\t"
      "v_add_u32 v242, 14336, v240\n\t"
      "v_add_u32 v243, 21504, v240\n\t"
      "v_lshlrev_b32 v254, 4, v255\n\t"
      "s_mov_b64 s[84:85], %[part]\n\t"
      "s_and_b32 s85, s85, 0xffff\n\t"
      "s_mov_b32 s86, 22937600\n\t"
      "s_mov_b32 s87, 0x20000\n\t"
      "s_mov_b64 s[80:81], %[wop]\n\t"
      "s_add_u32 m0, %[pfd], 118272\n\t"
      "s_nop 0\n\t"
      "global_load_lds_dwordx4 v240, s[80:81]\n\t"
      "s_sleep 1\n\t"
      "s_add_u32 m0, %[pfd], 119328\n\t"
      "s_nop 0\n\t"
      "global_load_lds_dwordx4 v241, s[80:81]\n\t"
      "s_sleep 1\n\t"
      "s_add_u32 m0, %[pfd], 120384\n\t"
      "s_nop 0\n\t"
      "global_load_lds_dwordx4 v242, s[80:81]\n\t"
      "s_sleep 1\n\t"
      "s_add_u32 m0, %[pfd], 121440\n\t"
      "s_nop 0\n\t"
      "global_load_lds_dwordx4 v243, s[80:81]\n\t"
      "s_sleep 1\n\t"
      "s_add_u32 s80, s80, 114688\n\t"
      "s_addc_u32 s81, s81, 0\n\t"
      "s_add_u32 m0, %[pfd], 101376\n\t"
      "s_nop 0\n\t"
      "global_load_lds_dwordx4 v240, s[80:81]\n\t"
      "s_sleep 1\n\t"
      "s_add_u32 m0, %[pfd], 102432\n\t"
      "s_nop 0\n\t"
      "global_load_lds_dwordx4 v241, s[80:81]\n\t"
      "s_sleep 1\n\t"
      "s_add_u32 m0, %[pfd], 103488\n\t"
      "s_nop 0\n\t"
      "global_load_lds_dwordx4 v242, s[80:81]\n\t"
      "s_sleep 1\n\t"
      "s_add_u32 m0, %[pfd], 104544\n\t"
      "s_nop 0\n\t"
      "global_load_lds_dwordx4 v243, s[80:81]\n\t"
      "s_sleep 1\n\t"
      "s_add_u32 s80, s80, 114688\n\t"
      "s_addc_u32 s81, s81, 0\n\t"
      "s_add_u32 m0, %[pfd], 84480\n\t"
      "s_nop 0\n\t"
      "global_load_lds_dwordx4 v240, s[80:81]\n\t"
      "s_sleep 1\n\t"
      "s_add_u32 m0, %[pfd], 85536\n\t"
      "s_nop 0\n\t"
      "global_load_lds_dwordx4 v241, s[80:81]\n\t"
      "s_sleep 1\n\t"
      "s_add_u32 m0, %[pfd], 86592\n\t"
      "s_nop 0\n\t"
      "global_load_lds_dwordx4 v242, s[80:81]\n\t"
      "s_sleep 1\n\t"
      "s_add_u32 m0, %[pfd], 87648\n\t"
      "s_nop 0\n\t"
      "global_load_lds_dwordx4 v243, s[80:81]\n\t"
      "s_sleep 1\n\t"
      "v_min_u32 v236, 47, v255\n\t"
      "v_lshrrev_b32 v237, 3, v236\n\t"
      "v_and_b32 v236, 7, v236\n\t"
      "v_mul_u32_u24 v236, 896, v236\n\t"
      "v_add_u32 v237, %[gw], v237\n\t"
      "v_subrev_u32 v238, 7, v237\n\t"
      "v_min_u32 v237, v237, v238\n\t"
      "v_lshl_add_u32 v236, v237, 7, v236\n\t"
      "global_load_dword v236, v236, %[wot]\n\t"
      "v_lshrrev_b32 v252, 2, v255\n\t"
      "v_mul_u32_u24 v252, 1056, v252\n\t"
      "v_and_b32 v253, 3, v255\n\t"
      "v_lshl_add_u32 v252, v253, 3, v252\n\t"
      "v_add_u32 v253, %[a2], v252\n\t"
      "v_add_u32 v252, 67584, v253\n\t"
      "v_xor_b32 v253, v252, v253\n\t"
      "v_add_u32 v250, %[oimg], v254\n\t"
      "v_add_u32 v251, 49152, v250\n\t"
      "s_mov_b32 s86, 0\n\t"
      "s_mov_b32 s88, %[po]\n\t"
      "s_add_u32 s91, %[pb], 716800\n\t"
      "v_mov_b32 v248, 80\n\t"
      "ds_read_b64 v[246:247], v248\n\t"
      "v_and_b32 v244, 15, v255\n\t"
      "v_cmp_gt_u32 vcc, 2, v244\n\t"
      "v_lshrrev_b32 v245, 4, v255\n\t"
      "v_lshl_add_u32 v244, v245, 1, v244\n\t"
      "v_lshlrev_b32 v244, 4, v244\n\t"
      "v_add_u32 v248, 3072, v244\n\t"
      "v_mov_b32 v255, 0x4000000\n\t"
      "v_cndmask_b32 v255, v255, v248, vcc\n\t"
      "s_waitcnt lgkmcnt(0)\n\t"
      "v_readfirstlane_b32 s82, v246\n\t"
      "v_readfirstlane_b32 s83, v247\n\t"
      "s_barrier\n\t"
      "ds_read_b128 v[0:3], v250 offset:0\n\t"
      "ds_read_b128 v[12:15], v250 offset:1024\n\t"
      "ds_read_b128 v[24:27], v250 offset:2048\n\t"
      "ds_read_b128 v[36:39], v250 offset:3072\n\t"
      "ds_read_b128 v[96:99], v251 offset:0\n\t"
      "ds_read_b128 v[108:111], v251 offset:1024\n\t"
      "ds_read_b128 v[120:123], v251 offset:2048\n\t"
      "ds_read_b128 v[132:135], v251 offset:3072\n\t"
      "ds_read_b128 v[4:7], v250 offset:8192\n\t"
      "ds_read_b128 v[16:19], v250 offset:9216\n\t"
      "ds_read_b128 v[28:31], v250 offset:10240\n\t"
      "ds_read_b128 v[40:43], v250 offset:11264\n\t"
      "ds_read_b128 v[100:103], v251 offset:8192\n\t"
      "ds_read_b128 v[112:115], v251 offset:9216\n\t"
      "ds_read_b128 v[124:127], v251 offset:10240\n\t"
      "ds_read_b128 v[136:139], v251 offset:11264\n\t"
      "ds_read_b128 v[8:11], v250 offset:16384\n\t"
      "ds_read_b128 v[20:23], v250 offset:17408\n\t"
      "ds_read_b128 v[32:35], v250 offset:18432\n\t"
      "ds_read_b128 v[44:47], v250 offset:19456\n\t"
      "ds_read_b128 v[104:107], v251 offset:16384\n\t"
      "ds_read_b128 v[116:119], v251 offset:17408\n\t"
      "ds_read_b128 v[128:131], v251 offset:18432\n\t"
      "ds_read_b128 v[140:143], v251 offset:19456\n\t"
      "s_barrier\n\t"
      "ds_read_b128 v[48:51], v250 offset:4096\n\t"
      "ds_read_b128 v[60:63], v250 offset:5120\n\t"
      "ds_read_b128 v[72:75], v250 offset:6144\n\t"
      "ds_read_b128 v[84:87], v250 offset:7168\n\t"
      "ds_read_b128 v[144:147], v251 offset:4096\n\t"
      "ds_read_b128 v[156:159], v251 offset:5120\n\t"
      "ds_read_b128 v[168:171], v251 offset:6144\n\t"
      "ds_read_b128 v[180:183], v251 offset:7168\n\t"
      "ds_read_b128 v[52:55], v250 offset:12288\n\t"
      "ds_read_b128 v[64:67], v250 offset:13312\n\t"
      "ds_read_b128 v[76:79], v250 offset:14336\n\t"
      "ds_read_b128 v[88:91], v250 offset:15360\n\t"
      "ds_read_b128 v[148:151], v251 offset:12288\n\t"
      "ds_read_b128 v[160:163], v251 offset:13312\n\t"
      "ds_read_b128 v[172:175], v251 offset:14336\n\t"
      "ds_read_b128 v[184:187], v251 offset:15360\n\t"
      "s_barrier\n\t"
      "ds_read_b128 v[56:59], v250 offset:20480\n\t"
      "ds_read_b128 v[68:71], v250 offset:21504\n\t"
      "ds_read_b128 v[80:83], v250 offset:22528\n\t"
      "ds_read_b128 v[92:95], v250 offset:23552\n\t"
      "ds_read_b128 v[152:155], v251 offset:20480\n\t"
      "ds_read_b128 v[164:167], v251 offset:21504\n\t"
      "ds_read_b128 v[176:179], v251 offset:22528\n\t"
      "ds_read_b128 v[188:191], v251 offset:23552\n\t"
      "s_waitcnt vmcnt(0) lgkmcnt(0)\n\t"
      "s_barrier\n\t"
      "ds_read_b64_tr_b16 v[216:217], v252 offset:50688\n\t"
      "ds_read_b64_tr_b16 v[218:219], v252 offset:50816\n\t"
      "ds_read_b64_tr_b16 v[220:221], v252 offset:50944\n\t"
      "ds_read_b64_tr_b16 v[222:223], v252 offset:51072\n\t"
      "ds_read_b64_tr_b16 v[224:225], v252 offset:51200\n\t"
      "ds_read_b64_tr_b16 v[226:227], v252 offset:51328\n\t"
      "ds_read_b64_tr_b16 v[228:229], v252 offset:51456\n\t"
      "ds_read_b64_tr_b16 v[230:231], v252 offset:51584\n\t"
      "s_mov_b32 s89, 6\n\t"
      "v_mov_b32 v250, s82\n\t"
      "v_mov_b32 v251, s83\n\t"
      "s_waitcnt lgkmcnt(4)\n\t"
      "v_mfma_f32_16x16x32_bf16 v[192:195], v[216:219], v[0:3], 0\n\t"
      "ds_read_b64_tr_b16 v[232:233], v252 offset:50720\n\t"
      "v_mfma_f32_16x16x32_bf16 v[192:195], v[220:223], v[12:15], v[192:195]\n\t"
      "ds_read_b64_tr_b16 v[234:235], v252 offset:50848\n\t"
      "s_waitcnt lgkmcnt(2)\n\t"
      "v_mfma_f32_16x16x32_bf16 v[192:195], v[224:227], v[24:27], v[192:195]\n\t"
      "ds_read_b64_tr_b16 v[236:237], v252 offset:50976\n\t"
      "v_mfma_f32_16x16x32_bf16 v[192:195], v[228:231], v[36:39], v[192:195]\n\t"
      "ds_read_b64_tr_b16 v[238:239], v252 offset:51104\n\t"
      "v_mfma_f32_16x16x32_bf16 v[196:199], v[228:231], v[40:43], 0\n\t"
      "ds_read_b64_tr_b16 v[240:241], v252 offset:51232\n\t"
      "v_mfma_f32_16x16x32_bf16 v[196:199], v[224:227], v[28:31], v[196:199]\n\t"
      "ds_read_b64_tr_b16 v[242:243], v252 offset:51360\n\t"
      "v_mfma_f32_16x16x32_bf16 v[196:199], v[220:223], v[16:19], v[196:199]\n\t"
      "ds_read_b64_tr_b16 v[244:245], v252 offset:51488\n\t"
      "v_mfma_f32_16x16x32_bf16 v[196:199], v[216:219], v[4:7], v[196:199]\n\t"
      "ds_read_b64_tr_b16 v[246:247], v252 offset:51616\n\t"
      "v_mfma_f32_16x16x32_bf16 v[200:203], v[216:219], v[8:11], 0\n\t"
      "v_mfma_f32_16x16x32_bf16 v[200:203], v[220:223], v[20:23], v[200:203]\n\t"
      "v_mfma_f32_16x16x32_bf16 v[200:203], v[224:227], v[32:35], v[200:203]\n\t"
      "v_mfma_f32_16x16x32_bf16 v[200:203], v[228:231], v[44:47], v[200:203]\n\t"
      "s_waitcnt lgkmcnt(4)\n\t"
      "v_mfma_f32_16x16x32_bf16 v[204:207], v[232:235], v[0:3], 0\n\t"
      "ds_read_b64_tr_b16 v[216:217], v252 offset:33792\n\t"
      "v_mfma_f32_16x16x32_bf16 v[204:207], v[236:239], v[12:15], v[204:207]\n\t"
      "ds_read_b64_tr_b16 v[218:219], v252 offset:33920\n\t"
      "s_waitcnt lgkmcnt(2)\n\t"
      "v_mfma_f32_16x16x32_bf16 v[204:207], v[240:243], v[24:27], v[204:207]\n\t"
      "ds_read_b64_tr_b16 v[220:221], v252 offset:34048\n\t"
      "v_mfma_f32_16x16x32_bf16 v[204:207], v[244:247], v[36:39], v[204:207]\n\t"
      "ds_read_b64_tr_b16 v[222:223], v252 offset:34176\n\t"
      "v_mfma_f32_16x16x32_bf16 v[208:211], v[244:247], v[40:43], 0\n\t"
      "ds_read_b64_tr_b16 v[224:225], v252 offset:34304\n\t"
      "v_mfma_f32_16x16x32_bf16 v[208:211], v[240:243], v[28:31], v[208:211]\n\t"
      "ds_read_b64_tr_b16 v[226:227], v252 offset:34432\n\t"
      "v_mfma_f32_16x16x32_bf16 v[208:211], v[236:239], v[16:19], v[208:211]\n\t"
      "ds_read_b64_tr_b16 v[228:229], v252 offset:34560\n\t"
      "v_mfma_f32_16x16x32_bf16 v[208:211], v[232:235], v[4:7], v[208:211]\n\t"
      "ds_read_b64_tr_b16 v[230:231], v252 offset:34688\n\t"
      "v_mfma_f32_16x16x32_bf16 v[212:215], v[232:235], v[8:11], 0\n\t"
      "v_mfma_f32_16x16x32_bf16 v[212:215], v[236:239], v[20:23], v[212:215]\n\t"
      "v_mfma_f32_16x16x32_bf16 v[212:215], v[240:243], v[32:35], v[212:215]\n\t"
      "v_mfma_f32_16x16x32_bf16 v[212:215], v[244:247], v[44:47], v[212:215]\n\t"
      "s_branch 13f\n\t"
      "5:\n\t"
      "s_waitcnt lgkmcnt(4)\n\t"
      "v_mfma_f32_16x16x32_bf16 v[192:195], v[216:219], v[0:3], 0\n\t"
      "ds_read_b64_tr_b16 v[232:233], v252 offset:50720\n\t"
      "v_cvt_pk_bf16_f32 v209, v198, v199\n\t"
      "v_mfma_f32_16x16x32_bf16 v[192:195], v[220:223], v[12:15], v[192:195]\n\t"
      "ds_read_b64_tr_b16 v[234:235], v252 offset:50848\n\t"
      "v_add3_u32 v248, v204, v206, s82\n\t"
      "s_waitcnt lgkmcnt(2)\n\t"
      "v_mfma_f32_16x16x32_bf16 v[192:195], v[224:227], v[24:27], v[192:195]\n\t"
      "ds_read_b64_tr_b16 v[236:237], v252 offset:50976\n\t"
      "v_cvt_pk_bf16_f32 v215, v214, v215\n\t"
      "v_mfma_f32_16x16x32_bf16 v[192:195], v[228:231], v[36:39], v[192:195]\n\t"
      "ds_read_b64_tr_b16 v[238:239], v252 offset:51104\n\t"
      "v_cvt_pk_bf16_f32 v214, v212, v213\n\t"
      "v_mfma_f32_16x16x32_bf16 v[196:199], v[228:231], v[40:43], 0\n\t"
      "ds_read_b64_tr_b16 v[240:241], v252 offset:51232\n\t"
      "v_cvt_pk_bf16_f32 v212, v200, v201\n\t"
      "v_mfma_f32_16x16x32_bf16 v[196:199], v[224:227], v[28:31], v[196:199]\n\t"
      "ds_read_b64_tr_b16 v[242:243], v252 offset:51360\n\t"
      "v_cvt_pk_bf16_f32 v213, v202, v203\n\t"
      "v_mfma_f32_16x16x32_bf16 v[196:199], v[220:223], v[16:19], v[196:199]\n\t"
      "ds_read_b64_tr_b16 v[244:245], v252 offset:51488\n\t"
      "v_add3_u32 v249, v205, v207, s83\n\t"
      "v_mfma_f32_16x16x32_bf16 v[196:199], v[216:219], v[4:7], v[196:199]\n\t"
      "ds_read_b64_tr_b16 v[246:247], v252 offset:51616\n\t"
      "v_add3_u32 v248, v208, v210, v248\n\t"
      "v_mfma_f32_16x16x32_bf16 v[200:203], v[216:219], v[8:11], 0\n\t"
      "buffer_store_dwordx4 v[204:207], v254, s[84:87], s88 offen offset:0 sc1\n\t"
      "v_mfma_f32_16x16x32_bf16 v[200:203], v[220:223], v[20:23], v[200:203]\n\t"
      "v_add3_u32 v249, v209, v211, v249\n\t"
      "v_add3_u32 v248, v212, v214, v248\n\t"
      "v_mfma_f32_16x16x32_bf16 v[200:203], v[224:227], v[32:35], v[200:203]\n\t"
      "buffer_store_dwordx4 v[208:211], v254, s[84:87], s88 offen offset:1024 sc1\n\t"
      "v_mfma_f32_16x16x32_bf16 v[200:203], v[228:231], v[44:47], v[200:203]\n\t"
      "buffer_store_dwordx4 v[212:215], v254, s[84:87], s88 offen offset:2048 sc1\n\t"
      "s_waitcnt lgkmcnt(4)\n\t"
      "v_mfma_f32_16x16x32_bf16 v[204:207], v[232:235], v[0:3], 0\n\t"
      "ds_read_b64_tr_b16 v[216:217], v252 offset:33792\n\t"
      "v_add3_u32 v249, v213, v215, v249\n\t"
      "v_mfma_f32_16x16x32_bf16 v[204:207], v[236:239], v[12:15], v[204:207]\n\t"
      "ds_read_b64_tr_b16 v[218:219], v252 offset:33920\n\t"
      "v_permlane32_swap_b32 v248, v249\n\t"
      "s_waitcnt lgkmcnt(2)\n\t"
      "v_mfma_f32_16x16x32_bf16 v[204:207], v[240:243], v[24:27], v[204:207]\n\t"
      "ds_read_b64_tr_b16 v[220:221], v252 offset:34048\n\t"
      "v_add_u32 v248, v248, v249\n\t"
      "v_mfma_f32_16x16x32_bf16 v[204:207], v[244:247], v[36:39], v[204:207]\n\t"
      "ds_read_b64_tr_b16 v[222:223], v252 offset:34176\n\t"
      "v_add_u32_dpp v248, v248, v248 row_ror:8 row_mask:0xf bank_mask:0xf\n\t"
      "v_mfma_f32_16x16x32_bf16 v[208:211], v[244:247], v[40:43], 0\n\t"
      "ds_read_b64_tr_b16 v[224:225], v252 offset:34304\n\t"
      "v_add_u32_dpp v248, v248, v248 row_ror:4 row_mask:0xf bank_mask:0xf\n\t"
      "v_mfma_f32_16x16x32_bf16 v[208:211], v[240:243], v[28:31], v[208:211]\n\t"
      "ds_read_b64_tr_b16 v[226:227], v252 offset:34432\n\t"
      "v_add_u32_dpp v248, v248, v248 row_ror:2 row_mask:0xf bank_mask:0xf\n\t"
      "v_mfma_f32_16x16x32_bf16 v[208:211], v[236:239], v[16:19], v[208:211]\n\t"
      "ds_read_b64_tr_b16 v[228:229], v252 offset:34560\n\t"
      "v_add_u32_dpp v248, v248, v248 row_ror:1 row_mask:0xf bank_mask:0xf\n\t"
      "v_mfma_f32_16x16x32_bf16 v[208:211], v[232:235], v[4:7], v[208:211]\n\t"
      "ds_read_b64_tr_b16 v[230:231], v252 offset:34688\n\t"
      "v_xor_b32 v248, s82, v248\n\t"
      "v_mfma_f32_16x16x32_bf16 v[212:215], v[232:235], v[8:11], 0\n\t"
      "buffer_store_dwordx4 v[248:251], v255, s[84:87], s88 offen sc1\n\t"
      "v_mfma_f32_16x16x32_bf16 v[212:215], v[236:239], v[20:23], v[212:215]\n\t"
      "v_mfma_f32_16x16x32_bf16 v[212:215], v[240:243], v[32:35], v[212:215]\n\t"
      "v_mfma_f32_16x16x32_bf16 v[212:215], v[244:247], v[44:47], v[212:215]\n\t"
      "13:\n\t"
      "s_waitcnt lgkmcnt(4)\n\t"
      "v_mfma_f32_16x16x32_bf16 v[192:195], v[216:219], v[48:51], v[192:195]\n\t"
      "ds_read_b64_tr_b16 v[232:233], v252 offset:33824\n\t"
      "v_mfma_f32_16x16x32_bf16 v[192:195], v[220:223], v[60:63], v[192:195]\n\t"
      "ds_read_b64_tr_b16 v[234:235], v252 offset:33952\n\t"
      "s_waitcnt lgkmcnt(2)\n\t"
      "v_mfma_f32_16x16x32_bf16 v[192:195], v[224:227], v[72:75], v[192:195]\n\t"
      "ds_read_b64_tr_b16 v[236:237], v252 offset:34080\n\t"
      "v_mfma_f32_16x16x32_bf16 v[192:195], v[228:231], v[84:87], v[192:195]\n\t"
      "ds_read_b64_tr_b16 v[238:239], v252 offset:34208\n\t"
      "v_mfma_f32_16x16x32_bf16 v[196:199], v[228:231], v[88:91], v[196:199]\n\t"
      "ds_read_b64_tr_b16 v[240:241], v252 offset:34336\n\t"
      "v_mfma_f32_16x16x32_bf16 v[196:199], v[224:227], v[76:79], v[196:199]\n\t"
      "ds_read_b64_tr_b16 v[242:243], v252 offset:34464\n\t"
      "v_mfma_f32_16x16x32_bf16 v[196:199], v[220:223], v[64:67], v[196:199]\n\t"
      "ds_read_b64_tr_b16 v[244:245], v252 offset:34592\n\t"
      "v_mfma_f32_16x16x32_bf16 v[196:199], v[216:219], v[52:55], v[196:199]\n\t"
      "ds_read_b64_tr_b16 v[246:247], v252 offset:34720\n\t"
      "v_mfma_f32_16x16x32_bf16 v[200:203], v[216:219], v[56:59], v[200:203]\n\t"
      "s_add_u32 s88, s88, 102400\n\t"
      "s_cmp_eq_u32 s88, s91\n\t"
      "v_mfma_f32_16x16x32_bf16 v[200:203], v[220:223], v[68:71], v[200:203]\n\t"
      "s_cselect_b32 s88, %[pb], s88\n\t"
      "v_mfma_f32_16x16x32_bf16 v[200:203], v[224:227], v[80:83], v[200:203]\n\t"
      "s_mov_b32 s86, 22937600\n\t"
      "v_mfma_f32_16x16x32_bf16 v[200:203], v[228:231], v[92:95], v[200:203]\n\t"
      "s_waitcnt lgkmcnt(4)\n\t"
      "s_barrier\n\t"
      "v_mfma_f32_16x16x32_bf16 v[204:207], v[232:235], v[48:51], v[204:207]\n\t"
      "ds_read_b64_tr_b16 v[216:217], v252 offset:16896\n\t"
      "v_mfma_f32_16x16x32_bf16 v[204:207], v[236:239], v[60:63], v[204:207]\n\t"
      "ds_read_b64_tr_b16 v[218:219], v252 offset:17024\n\t"
      "s_waitcnt lgkmcnt(2)\n\t"
      "v_mfma_f32_16x16x32_bf16 v[204:207], v[240:243], v[72:75], v[204:207]\n\t"
      "ds_read_b64_tr_b16 v[220:221], v252 offset:17152\n\t"
      "v_mfma_f32_16x16x32_bf16 v[204:207], v[244:247], v[84:87], v[204:207]\n\t"
      "ds_read_b64_tr_b16 v[222:223], v252 offset:17280\n\t"
      "v_mfma_f32_16x16x32_bf16 v[208:211], v[244:247], v[88:91], v[208:211]\n\t"
      "ds_read_b64_tr_b16 v[224:225], v252 offset:17408\n\t"
      "v_mfma_f32_16x16x32_bf16 v[208:211], v[240:243], v[76:79], v[208:211]\n\t"
      "ds_read_b64_tr_b16 v[226:227], v252 offset:17536\n\t"
      "v_mfma_f32_16x16x32_bf16 v[208:211], v[236:239], v[64:67], v[208:211]\n\t"
      "ds_read_b64_tr_b16 v[228:229], v252 offset:17664\n\t"
      "v_mfma_f32_16x16x32_bf16 v[208:211], v[232:235], v[52:55], v[208:211]\n\t"
      "ds_read_b64_tr_b16 v[230:231], v252 offset:17792\n\t"
      "v_mfma_f32_16x16x32_bf16 v[212:215], v[232:235], v[56:59], v[212:215]\n\t"
      "v_mfma_f32_16x16x32_bf16 v[212:215], v[236:239], v[68:71], v[212:215]\n\t"
      "v_mfma_f32_16x16x32_bf16 v[212:215], v[240:243], v[80:83], v[212:215]\n\t"
      "v_mfma_f32_16x16x32_bf16 v[212:215], v[244:247], v[92:95], v[212:215]\n\t"
      "s_waitcnt lgkmcnt(4)\n\t"
      "v_mfma_f32_16x16x32_bf16 v[192:195], v[216:219], v[96:99], v[192:195]\n\t"
      "ds_read_b64_tr_b16 v[232:233], v252 offset:16928\n\t"
      "v_mfma_f32_16x16x32_bf16 v[192:195], v[220:223], v[108:111], v[192:195]\n\t"
      "ds_read_b64_tr_b16 v[234:235], v252 offset:17056\n\t"
      "s_waitcnt lgkmcnt(2)\n\t"
      "v_mfma_f32_16x16x32_bf16 v[192:195], v[224:227], v[120:123], v[192:195]\n\t"
      "ds_read_b64_tr_b16 v[236:237], v252 offset:17184\n\t"
      "v_mfma_f32_16x16x32_bf16 v[192:195], v[228:231], v[132:135], v[192:195]\n\t"
      "ds_read_b64_tr_b16 v[238:239], v252 offset:17312\n\t"
      "v_mfma_f32_16x16x32_bf16 v[196:199], v[228:231], v[136:139], v[196:199]\n\t"
      "ds_read_b64_tr_b16 v[240:241], v252 offset:17440\n\t"
      "v_mfma_f32_16x16x32_bf16 v[196:199], v[224:227], v[124:127], v[196:199]\n\t"
      "ds_read_b64_tr_b16 v[242:243], v252 offset:17568\n\t"
      "v_mfma_f32_16x16x32_bf16 v[196:199], v[220:223], v[112:115], v[196:199]\n\t"
      "ds_read_b64_tr_b16 v[244:245], v252 offset:17696\n\t"
      "v_mfma_f32_16x16x32_bf16 v[196:199], v[216:219], v[100:103], v[196:199]\n\t"
      "ds_read_b64_tr_b16 v[246:247], v252 offset:17824\n\t"
      "v_mfma_f32_16x16x32_bf16 v[200:203], v[216:219], v[104:107], v[200:203]\n\t"
      "v_mfma_f32_16x16x32_bf16 v[200:203], v[220:223], v[116:119], v[200:203]\n\t"
      "v_mfma_f32_16x16x32_bf16 v[200:203], v[224:227], v[128:131], v[200:203]\n\t"
      "v_mfma_f32_16x16x32_bf16 v[200:203], v[228:231], v[140:143], v[200:203]\n\t"
      "s_waitcnt lgkmcnt(4)\n\t"
      "v_mfma_f32_16x16x32_bf16 v[204:207], v[232:235], v[96:99], v[204:207]\n\t"
      "ds_read_b64_tr_b16 v[216:217], v252 offset:0\n\t"
      "v_mfma_f32_16x16x32_bf16 v[204:207], v[236:239], v[108:111], v[204:207]\n\t"
      "ds_read_b64_tr_b16 v[218:219], v252 offset:128\n\t"
      "s_waitcnt lgkmcnt(2)\n\t"
      "v_mfma_f32_16x16x32_bf16 v[204:207], v[240:243], v[120:123], v[204:207]\n\t"
      "ds_read_b64_tr_b16 v[220:221], v252 offset:256\n\t"
      "v_mfma_f32_16x16x32_bf16 v[204:207], v[244:247], v[132:135], v[204:207]\n\t"
      "ds_read_b64_tr_b16 v[222:223], v252 offset:384\n\t"
      "v_mfma_f32_16x16x32_bf16 v[208:211], v[244:247], v[136:139], v[208:211]\n\t"
      "ds_read_b64_tr_b16 v[224:225], v252 offset:512\n\t"
      "v_mfma_f32_16x16x32_bf16 v[208:211], v[240:243], v[124:127], v[208:211]\n\t"
      "ds_read_b64_tr_b16 v[226:227], v252 offset:640\n\t"
      "v_mfma_f32_16x16x32_bf16 v[208:211], v[236:239], v[112:115], v[208:211]\n\t"
      "ds_read_b64_tr_b16 v[228:229], v252 offset:768\n\t"
      "v_mfma_f32_16x16x32_bf16 v[208:211], v[232:235], v[100:103], v[208:211]\n\t"
      "ds_read_b64_tr_b16 v[230:231], v252 offset:896\n\t"
      "v_mfma_f32_16x16x32_bf16 v[212:215], v[232:235], v[104:107], v[212:215]\n\t"
      "v_mfma_f32_16x16x32_bf16 v[212:215], v[236:239], v[116:119], v[212:215]\n\t"
      "v_mfma_f32_16x16x32_bf16 v[212:215], v[240:243], v[128:131], v[212:215]\n\t"
      "v_mfma_f32_16x16x32_bf16 v[212:215], v[244:247], v[140:143], v[212:215]\n\t"
      "s_waitcnt lgkmcnt(4)\n\t"
      "v_mfma_f32_16x16x32_bf16 v[192:195], v[216:219], v[144:147], v[192:195]\n\t"
      "ds_read_b64_tr_b16 v[232:233], v252 offset:32\n\t"
      "v_mfma_f32_16x16x32_bf16 v[192:195], v[220:223], v[156:159], v[192:195]\n\t"
      "ds_read_b64_tr_b16 v[234:235], v252 offset:160\n\t"
      "s_waitcnt lgkmcnt(2)\n\t"
      "v_mfma_f32_16x16x32_bf16 v[192:195], v[224:227], v[168:171], v[192:195]\n\t"
      "ds_read_b64_tr_b16 v[236:237], v252 offset:288\n\t"
      "v_mfma_f32_16x16x32_bf16 v[192:195], v[228:231], v[180:183], v[192:195]\n\t"
      "ds_read_b64_tr_b16 v[238:239], v252 offset:416\n\t"
      "v_mfma_f32_16x16x32_bf16 v[196:199], v[228:231], v[184:187], v[196:199]\n\t"
      "ds_read_b64_tr_b16 v[240:241], v252 offset:544\n\t"
      "v_mfma_f32_16x16x32_bf16 v[196:199], v[224:227], v[172:175], v[196:199]\n\t"
      "ds_read_b64_tr_b16 v[242:243], v252 offset:672\n\t"
      "v_mfma_f32_16x16x32_bf16 v[196:199], v[220:223], v[160:163], v[196:199]\n\t"
      "ds_read_b64_tr_b16 v[244:245], v252 offset:800\n\t"
      "v_mfma_f32_16x16x32_bf16 v[196:199], v[216:219], v[148:151], v[196:199]\n\t"
      "ds_read_b64_tr_b16 v[246:247], v252 offset:928\n\t"
      "v_mfma_f32_16x16x32_bf16 v[200:203], v[216:219], v[152:155], v[200:203]\n\t"
      "v_xor_b32 v252, v253, v252\n\t"
      "v_mfma_f32_16x16x32_bf16 v[200:203], v[220:223], v[164:167], v[200:203]\n\t"
      "s_sub_u32 s89, s89, 1\n\t"
      "s_cmp_lg_u32 s89, 0\n\t"
      "v_mfma_f32_16x16x32_bf16 v[200:203], v[224:227], v[176:179], v[200:203]\n\t"
      "v_mfma_f32_16x16x32_bf16 v[200:203], v[228:231], v[188:191], v[200:203]\n\t"
      "s_waitcnt lgkmcnt(4)\n\t"
      "s_barrier\n\t"
      "v_mfma_f32_16x16x32_bf16 v[204:207], v[232:235], v[144:147], v[204:207]\n\t"
      "ds_read_b64_tr_b16 v[216:217], v252 offset:50688\n\t"
      "v_mfma_f32_16x16x32_bf16 v[204:207], v[236:239], v[156:159], v[204:207]\n\t"
      "ds_read_b64_tr_b16 v[218:219], v252 offset:50816\n\t"
      "s_waitcnt lgkmcnt(2)\n\t"
      "v_mfma_f32_16x16x32_bf16 v[204:207], v[240:243], v[168:171], v[204:207]\n\t"
      "ds_read_b64_tr_b16 v[220:221], v252 offset:50944\n\t"
      "v_mfma_f32_16x16x32_bf16 v[204:207], v[244:247], v[180:183], v[204:207]\n\t"
      "ds_read_b64_tr_b16 v[222:223], v252 offset:51072\n\t"
      "v_mfma_f32_16x16x32_bf16 v[208:211], v[244:247], v[184:187], v[208:211]\n\t"
      "ds_read_b64_tr_b16 v[224:225], v252 offset:51200\n\t"
      "v_mfma_f32_16x16x32_bf16 v[208:211], v[240:243], v[172:175], v[208:211]\n\t"
      "ds_read_b64_tr_b16 v[226:227], v252 offset:51328\n\t"
      "v_mfma_f32_16x16x32_bf16 v[208:211], v[236:239], v[160:163], v[208:211]\n\t"
      "ds_read_b64_tr_b16 v[228:229], v252 offset:51456\n\t"
      "v_mfma_f32_16x16x32_bf16 v[208:211], v[232:235], v[148:151], v[208:211]\n\t"
      "ds_read_b64_tr_b16 v[230:231], v252 offset:51584\n\t"
      "v_cvt_pk_bf16_f32 v207, v206, v207\n\t"
      "v_mfma_f32_16x16x32_bf16 v[212:215], v[232:235], v[152:155], v[212:215]\n\t"
      "v_cvt_pk_bf16_f32 v206, v204, v205\n\t"
      "v_cvt_pk_bf16_f32 v204, v192, v193\n\t"
      "v_mfma_f32_16x16x32_bf16 v[212:215], v[236:239], v[164:167], v[212:215]\n\t"
      "v_cvt_pk_bf16_f32 v205, v194, v195\n\t"
      "v_mfma_f32_16x16x32_bf16 v[212:215], v[240:243], v[176:179], v[212:215]\n\t"
      "v_cvt_pk_bf16_f32 v211, v210, v211\n\t"
      "v_mfma_f32_16x16x32_bf16 v[212:215], v[244:247], v[188:191], v[212:215]\n\t"
      "v_cvt_pk_bf16_f32 v210, v208, v209\n\t"
      "v_cvt_pk_bf16_f32 v208, v196, v197\n\t"
      "s_cbranch_scc1 5b\n\t"
      "s_waitcnt lgkmcnt(4)\n\t"
      "v_mfma_f32_16x16x32_bf16 v[192:195], v[216:219], v[0:3], 0\n\t"
      "ds_read_b64_tr_b16 v[232:233], v252 offset:50720\n\t"
      "v_cvt_pk_bf16_f32 v209, v198, v199\n\t"
      "v_mfma_f32_16x16x32_bf16 v[192:195], v[220:223], v[12:15], v[192:195]\n\t"
      "ds_read_b64_tr_b16 v[234:235], v252 offset:50848\n\t"
      "v_add3_u32 v248, v204, v206, s82\n\t"
      "s_waitcnt lgkmcnt(2)\n\t"
      "v_mfma_f32_16x16x32_bf16 v[192:195], v[224:227], v[24:27], v[192:195]\n\t"
      "ds_read_b64_tr_b16 v[236:237], v252 offset:50976\n\t"
      "v_cvt_pk_bf16_f32 v215, v214, v215\n\t"
      "v_mfma_f32_16x16x32_bf16 v[192:195], v[228:231], v[36:39], v[192:195]\n\t"
      "ds_read_b64_tr_b16 v[238:239], v252 offset:51104\n\t"
      "v_cvt_pk_bf16_f32 v214, v212, v213\n\t"
      "v_mfma_f32_16x16x32_bf16 v[196:199], v[228:231], v[40:43], 0\n\t"
      "ds_read_b64_tr_b16 v[240:241], v252 offset:51232\n\t"
      "v_cvt_pk_bf16_f32 v212, v200, v201\n\t"
      "v_mfma_f32_16x16x32_bf16 v[196:199], v[224:227], v[28:31], v[196:199]\n\t"
      "ds_read_b64_tr_b16 v[242:243], v252 offset:51360\n\t"
      "v_cvt_pk_bf16_f32 v213, v202, v203\n\t"
      "v_mfma_f32_16x16x32_bf16 v[196:199], v[220:223], v[16:19], v[196:199]\n\t"
      "ds_read_b64_tr_b16 v[244:245], v252 offset:51488\n\t"
      "v_add3_u32 v249, v205, v207, s83\n\t"
      "v_mfma_f32_16x16x32_bf16 v[196:199], v[216:219], v[4:7], v[196:199]\n\t"
      "ds_read_b64_tr_b16 v[246:247], v252 offset:51616\n\t"
      "v_add3_u32 v248, v208, v210, v248\n\t"
      "v_mfma_f32_16x16x32_bf16 v[200:203], v[216:219], v[8:11], 0\n\t"
      "buffer_store_dwordx4 v[204:207], v254, s[84:87], s88 offen offset:0 sc1\n\t"
      "v_mfma_f32_16x16x32_bf16 v[200:203], v[220:223], v[20:23], v[200:203]\n\t"
      "v_add3_u32 v249, v209, v211, v249\n\t"
      "v_add3_u32 v248, v212, v214, v248\n\t"
      "v_mfma_f32_16x16x32_bf16 v[200:203], v[224:227], v[32:35], v[200:203]\n\t"
      "buffer_store_dwordx4 v[208:211], v254, s[84:87], s88 offen offset:1024 sc1\n\t"
      "v_mfma_f32_16x16x32_bf16 v[200:203], v[228:231], v[44:47], v[200:203]\n\t"
      "buffer_store_dwordx4 v[212:215], v254, s[84:87], s88 offen offset:2048 sc1\n\t"
      "s_waitcnt lgkmcnt(4)\n\t"
      "v_mfma_f32_16x16x32_bf16 v[204:207], v[232:235], v[0:3], 0\n\t"
      "ds_read_b64_tr_b16 v[216:217], v252 offset:33792\n\t"
      "v_add3_u32 v249, v213, v215, v249\n\t"
      "v_mfma_f32_16x16x32_bf16 v[204:207], v[236:239], v[12:15], v[204:207]\n\t"
      "ds_read_b64_tr_b16 v[218:219], v252 offset:33920\n\t"
      "v_permlane32_swap_b32 v248, v249\n\t"
      "s_waitcnt lgkmcnt(2)\n\t"
      "v_mfma_f32_16x16x32_bf16 v[204:207], v[240:243], v[24:27], v[204:207]\n\t"
      "ds_read_b64_tr_b16 v[220:221], v252 offset:34048\n\t"
      "v_add_u32 v248, v248, v249\n\t"
      "v_mfma_f32_16x16x32_bf16 v[204:207], v[244:247], v[36:39], v[204:207]\n\t"
      "ds_read_b64_tr_b16 v[222:223], v252 offset:34176\n\t"
      "v_add_u32_dpp v248, v248, v248 row_ror:8 row_mask:0xf bank_mask:0xf\n\t"
      "v_mfma_f32_16x16x32_bf16 v[208:211], v[244:247], v[40:43], 0\n\t"
      "ds_read_b64_tr_b16 v[224:225], v252 offset:34304\n\t"
      "v_add_u32_dpp v248, v248, v248 row_ror:4 row_mask:0xf bank_mask:0xf\n\t"
      "v_mfma_f32_16x16x32_bf16 v[208:211], v[240:243], v[28:31], v[208:211]\n\t"
      "ds_read_b64_tr_b16 v[226:227], v252 offset:34432\n\t"
      "v_add_u32_dpp v248, v248, v248 row_ror:2 row_mask:0xf bank_mask:0xf\n\t"
      "v_mfma_f32_16x16x32_bf16 v[208:211], v[236:239], v[16:19], v[208:211]\n\t"
      "ds_read_b64_tr_b16 v[228:229], v252 offset:34560\n\t"
      "v_add_u32_dpp v248, v248, v248 row_ror:1 row_mask:0xf bank_mask:0xf\n\t"
      "v_mfma_f32_16x16x32_bf16 v[208:211], v[232:235], v[4:7], v[208:211]\n\t"
      "ds_read_b64_tr_b16 v[230:231], v252 offset:34688\n\t"
      "v_xor_b32 v248, s82, v248\n\t"
      "v_mfma_f32_16x16x32_bf16 v[212:215], v[232:235], v[8:11], 0\n\t"
      "buffer_store_dwordx4 v[248:251], v255, s[84:87], s88 offen sc1\n\t"
      "v_mfma_f32_16x16x32_bf16 v[212:215], v[236:239], v[20:23], v[212:215]\n\t"
      "s_mov_b64 s[76:77], %[part]\n\t"
      "s_add_u32 s74, s76, %[dl0]\n\t"
      "s_addc_u32 s75, s77, 0\n\t"
      "v_mfma_f32_16x16x32_bf16 v[212:215], v[240:243], v[32:35], v[212:215]\n\t"
      "s_add_u32 s76, s76, %[dl1]\n\t"
      "s_addc_u32 s77, s77, 0\n\t"
      "v_mfma_f32_16x16x32_bf16 v[212:215], v[244:247], v[44:47], v[212:215]\n\t"
      "s_not_b64 s[78:79], s[82:83]\n\t"
      "s_sub_u32 s73, 1, %[own1]\n\t"
      "s_waitcnt lgkmcnt(4)\n\t"
      "v_mfma_f32_16x16x32_bf16 v[192:195], v[216:219], v[48:51], v[192:195]\n\t"
      "ds_read_b64_tr_b16 v[232:233], v252 offset:33824\n\t"
      "v_mfma_f32_16x16x32_bf16 v[192:195], v[220:223], v[60:63], v[192:195]\n\t"
      "ds_read_b64_tr_b16 v[234:235], v252 offset:33952\n\t"
      "s_waitcnt lgkmcnt(2)\n\t"
      "v_mfma_f32_16x16x32_bf16 v[192:195], v[224:227], v[72:75], v[192:195]\n\t"
      "ds_read_b64_tr_b16 v[236:237], v252 offset:34080\n\t"
      "v_mfma_f32_16x16x32_bf16 v[192:195], v[228:231], v[84:87], v[192:195]\n\t"
      "ds_read_b64_tr_b16 v[238:239], v252 offset:34208\n\t"
      "v_mfma_f32_16x16x32_bf16 v[196:199], v[228:231], v[88:91], v[196:199]\n\t"
      "ds_read_b64_tr_b16 v[240:241], v252 offset:34336\n\t"
      "v_mfma_f32_16x16x32_bf16 v[196:199], v[224:227], v[76:79], v[196:199]\n\t"
      "ds_read_b64_tr_b16 v[242:243], v252 offset:34464\n\t"
      "v_mfma_f32_16x16x32_bf16 v[196:199], v[220:223], v[64:67], v[196:199]\n\t"
      "ds_read_b64_tr_b16 v[244:245], v252 offset:34592\n\t"
      "v_mfma_f32_16x16x32_bf16 v[196:199], v[216:219], v[52:55], v[196:199]\n\t"
      "ds_read_b64_tr_b16 v[246:247], v252 offset:34720\n\t"
      "v_mfma_f32_16x16x32_bf16 v[200:203], v[216:219], v[56:59], v[200:203]\n\t"
      "s_add_u32 s88, s88, 102400\n\t"
      "s_cmp_eq_u32 s88, s91\n\t"
      "v_mfma_f32_16x16x32_bf16 v[200:203], v[220:223], v[68:71], v[200:203]\n\t"
      "s_cselect_b32 s88, %[pb], s88\n\t"
      "v_mfma_f32_16x16x32_bf16 v[200:203], v[224:227], v[80:83], v[200:203]\n\t"
      "v_lshrrev_b32 v42, 8, v254\n\t"
      "v_lshlrev_b32 v42, 5, v42\n\t"
      "v_mfma_f32_16x16x32_bf16 v[200:203], v[228:231], v[92:95], v[200:203]\n\t"
      "s_waitcnt lgkmcnt(4)\n\t"
      "s_barrier\n\t"
      "v_mfma_f32_16x16x32_bf16 v[204:207], v[232:235], v[48:51], v[204:207]\n\t"
      "ds_read_b64_tr_b16 v[216:217], v252 offset:16896\n\t"
      "v_mfma_f32_16x16x32_bf16 v[204:207], v[236:239], v[60:63], v[204:207]\n\t"
      "ds_read_b64_tr_b16 v[218:219], v252 offset:17024\n\t"
      "s_waitcnt lgkmcnt(2)\n\t"
      "v_mfma_f32_16x16x32_bf16 v[204:207], v[240:243], v[72:75], v[204:207]\n\t"
      "ds_read_b64_tr_b16 v[220:221], v252 offset:17152\n\t"
      "v_mfma_f32_16x16x32_bf16 v[204:207], v[244:247], v[84:87], v[204:207]\n\t"
      "ds_read_b64_tr_b16 v[222:223], v252 offset:17280\n\t"
      "s_load_dword s78, s[74:75], 3080 glc\n\t"
      "s_load_dword s79, s[76:77], 3084 glc\n\t"
      "v_mfma_f32_16x16x32_bf16 v[208:211], v[244:247], v[88:91], v[208:211]\n\t"
      "ds_read_b64_tr_b16 v[224:225], v252 offset:17408\n\t"
      "v_mfma_f32_16x16x32_bf16 v[208:211], v[240:243], v[76:79], v[208:211]\n\t"
      "ds_read_b64_tr_b16 v[226:227], v252 offset:17536\n\t"
      "v_mfma_f32_16x16x32_bf16 v[208:211], v[236:239], v[64:67], v[208:211]\n\t"
      "ds_read_b64_tr_b16 v[228:229], v252 offset:17664\n\t"
      "v_mfma_f32_16x16x32_bf16 v[208:211], v[232:235], v[52:55], v[208:211]\n\t"
      "ds_read_b64_tr_b16 v[230:231], v252 offset:17792\n\t"
      "v_mfma_f32_16x16x32_bf16 v[212:215], v[232:235], v[56:59], v[212:215]\n\t"
      "v_mfma_f32_16x16x32_bf16 v[212:215], v[236:239], v[68:71], v[212:215]\n\t"
      "v_mfma_f32_16x16x32_bf16 v[212:215], v[240:243], v[80:83], v[212:215]\n\t"
      "v_mfma_f32_16x16x32_bf16 v[212:215], v[244:247], v[92:95], v[212:215]\n\t"
      "s_waitcnt lgkmcnt(4)\n\t"
      "v_mfma_f32_16x16x32_bf16 v[192:195], v[216:219], v[96:99], v[192:195]\n\t"
      "ds_read_b64_tr_b16 v[232:233], v252 offset:16928\n\t"
      "v_mfma_f32_16x16x32_bf16 v[192:195], v[220:223], v[108:111], v[192:195]\n\t"
      "ds_read_b64_tr_b16 v[234:235], v252 offset:17056\n\t"
      "s_waitcnt lgkmcnt(2)\n\t"
      "v_mfma_f32_16x16x32_bf16 v[192:195], v[224:227], v[120:123], v[192:195]\n\t"
      "ds_read_b64_tr_b16 v[236:237], v252 offset:17184\n\t"
      "v_mfma_f32_16x16x32_bf16 v[192:195], v[228:231], v[132:135], v[192:195]\n\t"
      "ds_read_b64_tr_b16 v[238:239], v252 offset:17312\n\t"
      "v_mfma_f32_16x16x32_bf16 v[196:199], v[228:231], v[136:139], v[196:199]\n\t"
      "ds_read_b64_tr_b16 v[240:241], v252 offset:17440\n\t"
      "v_mfma_f32_16x16x32_bf16 v[196:199], v[224:227], v[124:127], v[196:199]\n\t"
      "ds_read_b64_tr_b16 v[242:243], v252 offset:17568\n\t"
      "v_mfma_f32_16x16x32_bf16 v[196:199], v[220:223], v[112:115], v[196:199]\n\t"
      "ds_read_b64_tr_b16 v[244:245], v252 offset:17696\n\t"
      "v_mfma_f32_16x16x32_bf16 v[196:199], v[216:219], v[100:103], v[196:199]\n\t"
      "ds_read_b64_tr_b16 v[246:247], v252 offset:17824\n\t"
      "v_mfma_f32_16x16x32_bf16 v[200:203], v[216:219], v[104:107], v[200:203]\n\t"
      "v_mfma_f32_16x16x32_bf16 v[200:203], v[220:223], v[116:119], v[200:203]\n\t"
      "v_mfma_f32_16x16x32_bf16 v[200:203], v[224:227], v[128:131], v[200:203]\n\t"
      "v_mfma_f32_16x16x32_bf16 v[200:203], v[228:231], v[140:143], v[200:203]\n\t"
      "s_waitcnt lgkmcnt(4)\n\t"
      "v_mfma_f32_16x16x32_bf16 v[204:207], v[232:235], v[96:99], v[204:207]\n\t"
      "ds_read_b64_tr_b16 v[216:217], v252 offset:0\n\t"
      "v_mfma_f32_16x16x32_bf16 v[204:207], v[236:239], v[108:111], v[204:207]\n\t"
      "ds_read_b64_tr_b16 v[218:219], v252 offset:128\n\t"
      "s_waitcnt lgkmcnt(2)\n\t"
      "v_mfma_f32_16x16x32_bf16 v[204:207], v[240:243], v[120:123], v[204:207]\n\t"
      "ds_read_b64_tr_b16 v[220:221], v252 offset:256\n\t"
      "v_mfma_f32_16x16x32_bf16 v[204:207], v[244:247], v[132:135], v[204:207]\n\t"
      "ds_read_b64_tr_b16 v[222:223], v252 offset:384\n\t"
      "s_cmp_lg_u32 s73, 0\n\t"
      "s_cbranch_scc1 11f\n\t"
      "s_cmp_lg_u64 s[78:79], s[82:83]\n\t"
      "s_cbranch_scc1 12f\n\t"
      "buffer_load_dwordx4 v[0:3], v254, s[84:87], %[dl0] offen offset:0 sc1\n\t"
      "buffer_load_dwordx4 v[4:7], v254, s[84:87], %[dl0] offen offset:1024 sc1\n\t"
      "buffer_load_dwordx4 v[8:11], v254, s[84:87], %[dl0] offen offset:2048 sc1\n\t"
      "buffer_load_dword v12, v42, s[84:87], %[dl0] offen offset:3072 sc1\n\t"
      "buffer_load_dwordx4 v[16:19], v254, s[84:87], %[dl1] offen offset:0 sc1\n\t"
      "buffer_load_dwordx4 v[20:23], v254, s[84:87], %[dl1] offen offset:1024 sc1\n\t"
      "buffer_load_dwordx4 v[24:27], v254, s[84:87], %[dl1] offen offset:2048 sc1\n\t"
      "buffer_load_dword v28, v42, s[84:87], %[dl1] offen offset:3072 sc1\n\t"
      "s_mov_b32 s73, 1\n\t"
      "s_branch 11f\n\t"
      "12:\n\t"
      "s_load_dword s78, s[74:75], 3080 glc\n\t"
      "s_load_dword s79, s[76:77], 3084 glc\n\t"
      "11:\n\t"
      "v_mfma_f32_16x16x32_bf16 v[208:211], v[244:247], v[136:139], v[208:211]\n\t"
      "ds_read_b64_tr_b16 v[224:225], v252 offset:512\n\t"
      "v_mfma_f32_16x16x32_bf16 v[208:211], v[240:243], v[124:127], v[208:211]\n\t"
      "ds_read_b64_tr_b16 v[226:227], v252 offset:640\n\t"
      "v_mfma_f32_16x16x32_bf16 v[208:211], v[236:239], v[112:115], v[208:211]\n\t"
      "ds_read_b64_tr_b16 v[228:229], v252 offset:768\n\t"
      "v_mfma_f32_16x16x32_bf16 v[208:211], v[232:235], v[100:103], v[208:211]\n\t"
      "ds_read_b64_tr_b16 v[230:231], v252 offset:896\n\t"
      "v_mfma_f32_16x16x32_bf16 v[212:215], v[232:235], v[104:107], v[212:215]\n\t"
      "v_mfma_f32_16x16x32_bf16 v[212:215], v[236:239], v[116:119], v[212:215]\n\t"
      "v_mfma_f32_16x16x32_bf16 v[212:215], v[240:243], v[128:131], v[212:215]\n\t"
      "v_mfma_f32_16x16x32_bf16 v[212:215], v[244:247], v[140:143], v[212:215]\n\t"
      "s_waitcnt lgkmcnt(4)\n\t"
      "v_mfma_f32_16x16x32_bf16 v[192:195], v[216:219], v[144:147], v[192:195]\n\t"
      "ds_read_b64_tr_b16 v[232:233], v252 offset:32\n\t"
      "v_mfma_f32_16x16x32_bf16 v[192:195], v[220:223], v[156:159], v[192:195]\n\t"
      "ds_read_b64_tr_b16 v[234:235], v252 offset:160\n\t"
      "s_waitcnt lgkmcnt(2)\n\t"
      "v_mfma_f32_16x16x32_bf16 v[192:195], v[224:227], v[168:171], v[192:195]\n\t"
      "ds_read_b64_tr_b16 v[236:237], v252 offset:288\n\t"
      "v_mfma_f32_16x16x32_bf16 v[192:195], v[228:231], v[180:183], v[192:195]\n\t"
      "ds_read_b64_tr_b16 v[238:239], v252 offset:416\n\t"
      "s_cmp_lg_u32 s73, 0\n\t"
      "s_cbranch_scc1 11f\n\t"
      "s_cmp_lg_u64 s[78:79], s[82:83]\n\t"
      "s_cbranch_scc1 12f\n\t"
      "buffer_load_dwordx4 v[0:3], v254, s[84:87], %[dl0] offen offset:0 sc1\n\t"
      "buffer_load_dwordx4 v[4:7], v254, s[84:87], %[dl0] offen offset:1024 sc1\n\t"
      "buffer_load_dwordx4 v[8:11], v254, s[84:87], %[dl0] offen offset:2048 sc1\n\t"
      "buffer_load_dword v12, v42, s[84:87], %[dl0] offen offset:3072 sc1\n\t"
      "buffer_load_dwordx4 v[16:19], v254, s[84:87], %[dl1] offen offset:0 sc1\n\t"
      "buffer_load_dwordx4 v[20:23], v254, s[84:87], %[dl1] offen offset:1024 sc1\n\t"
      "buffer_load_dwordx4 v[24:27], v254, s[84:87], %[dl1] offen offset:2048 sc1\n\t"
      "buffer_load_dword v28, v42, s[84:87], %[dl1] offen offset:3072 sc1\n\t"
      "s_mov_b32 s73, 1\n\t"
      "s_branch 11f\n\t"
      "12:\n\t"
      "s_load_dword s78, s[74:75], 3080 glc\n\t"
      "s_load_dword s79, s[76:77], 3084 glc\n\t"
      "11:\n\t"
      "v_mfma_f32_16x16x32_bf16 v[196:199], v[228:231], v[184:187], v[196:199]\n\t"
      "ds_read_b64_tr_b16 v[240:241], v252 offset:544\n\t"
      "v_mfma_f32_16x16x32_bf16 v[196:199], v[224:227], v[172:175], v[196:199]\n\t"
      "ds_read_b64_tr_b16 v[242:243], v252 offset:672\n\t"
      "v_mfma_f32_16x16x32_bf16 v[196:199], v[220:223], v[160:163], v[196:199]\n\t"
      "ds_read_b64_tr_b16 v[244:245], v252 offset:800\n\t"
      "v_mfma_f32_16x16x32_bf16 v[196:199], v[216:219], v[148:151], v[196:199]\n\t"
      "ds_read_b64_tr_b16 v[246:247], v252 offset:928\n\t"
      "v_mfma_f32_16x16x32_bf16 v[200:203], v[216:219], v[152:155], v[200:203]\n\t"
      "v_mfma_f32_16x16x32_bf16 v[200:203], v[220:223], v[164:167], v[200:203]\n\t"
      "v_mfma_f32_16x16x32_bf16 v[200:203], v[224:227], v[176:179], v[200:203]\n\t"
      "v_mfma_f32_16x16x32_bf16 v[200:203], v[228:231], v[188:191], v[200:203]\n\t"
      "v_lshrrev_b32 v84, 4, v254\n\t"
      "v_mov_b32 v52, %[srdy]\n\t"
      "s_waitcnt lgkmcnt(4)\n\t"
      "v_mfma_f32_16x16x32_bf16 v[204:207], v[232:235], v[144:147], v[204:207]\n\t"
      "v_add_u32 v53, %[sums], v254\n\t"
      "v_lshrrev_b32 v48, 2, v84\n\t"
      "v_mfma_f32_16x16x32_bf16 v[204:207], v[236:239], v[156:159], v[204:207]\n\t"
      "v_mul_u32_u24 v50, 1792, v48\n\t"
      "v_and_b32 v49, 3, v84\n\t"
      "s_waitcnt lgkmcnt(0)\n\t"
      "v_mfma_f32_16x16x32_bf16 v[204:207], v[240:243], v[168:171], v[204:207]\n\t"
      "v_lshl_add_u32 v50, v49, 4, v50\n\t"
      "v_bfrev_b32 v49, v49\n\t"
      "v_mfma_f32_16x16x32_bf16 v[204:207], v[244:247], v[180:183], v[204:207]\n\t"
      "s_cmp_lg_u32 s73, 0\n\t"
      "s_cbranch_scc1 11f\n\t"
      "s_cmp_lg_u64 s[78:79], s[82:83]\n\t"
      "s_cbranch_scc1 12f\n\t"
      "buffer_load_dwordx4 v[0:3], v254, s[84:87], %[dl0] offen offset:0 sc1\n\t"
      "buffer_load_dwordx4 v[4:7], v254, s[84:87], %[dl0] offen offset:1024 sc1\n\t"
      "buffer_load_dwordx4 v[8:11], v254, s[84:87], %[dl0] offen offset:2048 sc1\n\t"
      "buffer_load_dword v12, v42, s[84:87], %[dl0] offen offset:3072 sc1\n\t"
      "buffer_load_dwordx4 v[16:19], v254, s[84:87], %[dl1] offen offset:0 sc1\n\t"
      "buffer_load_dwordx4 v[20:23], v254, s[84:87], %[dl1] offen offset:1024 sc1\n\t"
      "buffer_load_dwordx4 v[24:27], v254, s[84:87], %[dl1] offen offset:2048 sc1\n\t"
      "buffer_load_dword v28, v42, s[84:87], %[dl1] offen offset:3072 sc1\n\t"
      "s_mov_b32 s73, 1\n\t"
      "s_branch 11f\n\t"
      "12:\n\t"
      "s_load_dword s78, s[74:75], 3080 glc\n\t"
      "s_load_dword s79, s[76:77], 3084 glc\n\t"
      "11:\n\t"
      "v_lshrrev_b32 v49, 26, v49\n\t"
      "v_add_lshl_u32 v51, v49, v48, 2\n\t"
      "v_mfma_f32_16x16x32_bf16 v[208:211], v[244:247], v[184:187], v[208:211]\n\t"
      "s_mov_b64 s[80:81], %[outp]\n\t"
      "v_bfe_u32 v40, v84, 2, 2\n\t"
      "v_mfma_f32_16x16x32_bf16 v[208:211], v[240:243], v[172:175], v[208:211]\n\t"
      "v_lshrrev_b32 v41, 4, v84\n\t"
      "v_cmp_eq_u32 vcc, v40, v41\n\t"
      "v_mfma_f32_16x16x32_bf16 v[208:211], v[236:239], v[160:163], v[208:211]\n\t"
      "ds_read_b64 v[54:55], v52\n\t"
      "v_and_b32 v40, 1, v84\n\t"
      "v_lshlrev_b32 v40, 4, v40\n\t"
      "v_mfma_f32_16x16x32_bf16 v[208:211], v[232:235], v[148:151], v[208:211]\n\t"
      "v_mov_b32 v41, 0x3f80\n\t"
      "v_lshlrev_b32 v40, v40, v41\n\t"
      "v_mfma_f32_16x16x32_bf16 v[212:215], v[232:235], v[152:155], v[212:215]\n\t"
      "v_cndmask_b32 v40, 0, v40, vcc\n\t"
      "v_and_b32 v41, 2, v84\n\t"
      "v_mfma_f32_16x16x32_bf16 v[212:215], v[236:239], v[164:167], v[212:215]\n\t"
      "v_cmp_eq_u32 vcc, 0, v41\n\t"
      "v_mov_b64 v[34:35], 0\n\t"
      "v_mfma_f32_16x16x32_bf16 v[212:215], v[240:243], v[176:179], v[212:215]\n\t"
      "v_mov_b64 v[36:37], 0\n\t"
      "v_cndmask_b32 v32, 0, v40, vcc\n\t"
      "v_mfma_f32_16x16x32_bf16 v[212:215], v[244:247], v[188:191], v[212:215]\n\t"
      "v_cndmask_b32 v33, v40, 0, vcc\n\t"
      "v_mov_b64 v[38:39], v[32:33]\n\t"
      "s_cmp_lg_u32 %[own1], 0\n\t"
      "s_cbranch_scc1 7f\n\t"
      "s_nop 7\n\t"
      "v_cvt_pk_bf16_f32 v216, v192, v193\n\t"
      "v_cvt_pk_bf16_f32 v217, v194, v195\n\t"
      "v_cvt_pk_bf16_f32 v218, v204, v205\n\t"
      "v_cvt_pk_bf16_f32 v219, v206, v207\n\t"
      "buffer_store_dwordx4 v[216:219], v254, s[84:87], s88 offen offset:0 sc1\n\t"
      "v_cvt_pk_bf16_f32 v220, v196, v197\n\t"
      "v_cvt_pk_bf16_f32 v221, v198, v199\n\t"
      "v_cvt_pk_bf16_f32 v222, v208, v209\n\t"
      "v_cvt_pk_bf16_f32 v223, v210, v211\n\t"
      "buffer_store_dwordx4 v[220:223], v254, s[84:87], s88 offen offset:1024 sc1\n\t"
      "v_cvt_pk_bf16_f32 v224, v200, v201\n\t"
      "v_cvt_pk_bf16_f32 v225, v202, v203\n\t"
      "v_cvt_pk_bf16_f32 v226, v212, v213\n\t"
      "v_cvt_pk_bf16_f32 v227, v214, v215\n\t"
      "buffer_store_dwordx4 v[224:227], v254, s[84:87], s88 offen offset:2048 sc1\n\t"
      "v_add3_u32 v248, v216, v218, s82\n\t"
      "v_add3_u32 v249, v217, v219, s83\n\t"
      "v_add3_u32 v248, v220, v222, v248\n\t"
      "v_add3_u32 v249, v221, v223, v249\n\t"
      "v_add3_u32 v248, v224, v226, v248\n\t"
      "v_add3_u32 v249, v225, v227, v249\n\t"
      "s_nop 1\n\t"
      "v_permlane32_swap_b32 v248, v249\n\t"
      "v_add_u32 v248, v248, v249\n\t"
      "s_nop 1\n\t"
      "v_add_u32_dpp v248, v248, v248 row_ror:8 row_mask:0xf bank_mask:0xf\n\t"
      "s_nop 1\n\t"
      "v_add_u32_dpp v248, v248, v248 row_ror:4 row_mask:0xf bank_mask:0xf\n\t"
      "s_nop 1\n\t"
      "v_add_u32_dpp v248, v248, v248 row_ror:2 row_mask:0xf bank_mask:0xf\n\t"
      "s_nop 1\n\t"
      "v_add_u32_dpp v248, v248, v248 row_ror:1 row_mask:0xf bank_mask:0xf\n\t"
      "v_xor_b32 v248, s82, v248\n\t"
      "buffer_store_dwordx4 v[248:251], v255, s[84:87], s88 offen sc1\n\t"
      "s_waitcnt lgkmcnt(0)\n\t"
      "s_branch 9f\n\t"
      "7:\n\t"
      "s_mov_b32 s89, 0x200000\n\t"
      "s_cmp_lg_u32 s73, 0\n\t"
      "s_cbranch_scc1 4f\n\t"
      "8:\n\t"
      "s_waitcnt lgkmcnt(0)\n\t"
      "s_cmp_lg_u64 s[78:79], s[82:83]\n\t"
      "s_cbranch_scc0 6f\n\t"
      "s_load_dword s78, s[74:75], 3080 glc\n\t"
      "s_load_dword s79, s[76:77], 3084 glc\n\t"
      "s_sub_u32 s89, s89, 1\n\t"
      "s_cbranch_scc0 8b\n\t"
      "s_mov_b32 s89, 0\n\t"
      "6:\n\t"
      "buffer_load_dwordx4 v[0:3], v254, s[84:87], %[dl0] offen offset:0 sc1\n\t"
      "buffer_load_dwordx4 v[4:7], v254, s[84:87], %[dl0] offen offset:1024 sc1\n\t"
      "buffer_load_dwordx4 v[8:11], v254, s[84:87], %[dl0] offen offset:2048 sc1\n\t"
      "buffer_load_dword v12, v42, s[84:87], %[dl0] offen offset:3072 sc1\n\t"
      "buffer_load_dwordx4 v[16:19], v254, s[84:87], %[dl1] offen offset:0 sc1\n\t"
      "buffer_load_dwordx4 v[20:23], v254, s[84:87], %[dl1] offen offset:1024 sc1\n\t"
      "buffer_load_dwordx4 v[24:27], v254, s[84:87], %[dl1] offen offset:2048 sc1\n\t"
      "buffer_load_dword v28, v42, s[84:87], %[dl1] offen offset:3072 sc1\n\t"
      "4:\n\t"
      "s_mov_b32 s88, s89\n\t"
      "s_waitcnt lgkmcnt(0)\n\t"
      "v_min_u32 v54, v54, v55\n\t"
      "v_cmp_le_u32 vcc, 1, v54\n\t"
      "s_cbranch_vccnz 3f\n\t"
      "s_mov_b32 s89, 0x1000000\n\t"
      "1:\n\t"
      "ds_read_b64 v[96:97], v52\n\t"
      "s_waitcnt lgkmcnt(0)\n\t"
      "v_min_u32 v96, v96, v97\n\t"
      "v_cmp_le_u32 vcc, 1, v96\n\t"
      "s_cbranch_vccnz 2f\n\t"
      "s_sleep 0\n\t"
      "s_sub_u32 s89, s89, 1\n\t"
      "s_cmp_lg_u32 s89, 0\n\t"
      "s_cbranch_scc1 1b\n\t"
      "2:\n\t"
      "3:\n\t"
      "ds_read_b128 v[100:103], v53 offset:0\n\t"
      "ds_read_b128 v[104:107], v53 offset:1024\n\t"
      "ds_read_b128 v[108:111], v53 offset:2048\n\t"
      "ds_read_b128 v[112:115], v53 offset:3072\n\t"
      "ds_read_b128 v[116:119], v53 offset:4096\n\t"
      "ds_read_b128 v[120:123], v53 offset:5120\n\t"
      "ds_read_b128 v[124:127], v53 offset:28672\n\t"
      "ds_read_b128 v[128:131], v53 offset:29696\n\t"
      "ds_read_b128 v[132:135], v53 offset:30720\n\t"
      "ds_read_b128 v[136:139], v53 offset:31744\n\t"
      "ds_read_b128 v[140:143], v53 offset:32768\n\t"
      "ds_read_b128 v[144:147], v53 offset:33792\n\t"
      "8:\n\t"
      "s_waitcnt vmcnt(0)\n\t"
      "v_add3_u32 v96, v0, v2, s82\n\t"
      "v_add3_u32 v98, v16, v18, s82\n\t"
      "v_add3_u32 v97, v1, v3, s83\n\t"
      "v_add3_u32 v99, v17, v19, s83\n\t"
      "v_add3_u32 v96, v4, v6, v96\n\t"
      "v_add3_u32 v98, v20, v22, v98\n\t"
      "v_add3_u32 v97, v5, v7, v97\n\t"
      "v_add3_u32 v99, v21, v23, v99\n\t"
      "v_add3_u32 v96, v8, v10, v96\n\t"
      "v_add3_u32 v98, v24, v26, v98\n\t"
      "v_add3_u32 v97, v9, v11, v97\n\t"
      "v_add3_u32 v99, v25, v27, v99\n\t"
      "s_nop 0\n\t"
      "v_permlane32_swap_b32 v96, v97\n\t"
      "v_permlane32_swap_b32 v98, v99\n\t"
      "v_add_u32 v96, v96, v97\n\t"
      "v_add_u32 v98, v98, v99\n\t"
      "s_nop 0\n\t"
      "v_add_u32_dpp v96, v96, v96 row_ror:8 row_mask:0xf bank_mask:0xf\n\t"
      "v_add_u32_dpp v98, v98, v98 row_ror:8 row_mask:0xf bank_mask:0xf\n\t"
      "s_nop 0\n\t"
      "v_add_u32_dpp v96, v96, v96 row_ror:4 row_mask:0xf bank_mask:0xf\n\t"
      "v_add_u32_dpp v98, v98, v98 row_ror:4 row_mask:0xf bank_mask:0xf\n\t"
      "s_nop 0\n\t"
      "v_add_u32_dpp v96, v96, v96 row_ror:2 row_mask:0xf bank_mask:0xf\n\t"
      "v_add_u32_dpp v98, v98, v98 row_ror:2 row_mask:0xf bank_mask:0xf\n\t"
      "s_nop 0\n\t"
      "v_add_u32_dpp v96, v96, v96 row_ror:1 row_mask:0xf bank_mask:0xf\n\t"
      "v_add_u32_dpp v98, v98, v98 row_ror:1 row_mask:0xf bank_mask:0xf\n\t"
      "v_xor_b32 v96, s82, v96\n\t"
      "v_xor_b32 v98, s82, v98\n\t"
      "v_cmp_ne_u32 vcc, v96, v12\n\t"
      "v_cmp_ne_u32 s[78:79], v98, v28\n\t"
      "s_or_b64 vcc, vcc, s[78:79]\n\t"
      "s_cbranch_scc0 6f\n\t"
      "s_sub_u32 s88, s88, 1\n\t"
      "s_cbranch_scc1 6f\n\t"
      "buffer_load_dwordx4 v[0:3], v254, s[84:87], %[dl0] offen offset:0 sc1\n\t"
      "buffer_load_dwordx4 v[4:7], v254, s[84:87], %[dl0] offen offset:1024 sc1\n\t"
      "buffer_load_dwordx4 v[8:11], v254, s[84:87], %[dl0] offen offset:2048 sc1\n\t"
      "buffer_load_dword v12, v42, s[84:87], %[dl0] offen offset:3072 sc1\n\t"
      "buffer_load_dwordx4 v[16:19], v254, s[84:87], %[dl1] offen offset:0 sc1\n\t"
      "buffer_load_dwordx4 v[20:23], v254, s[84:87], %[dl1] offen offset:1024 sc1\n\t"
      "buffer_load_dwordx4 v[24:27], v254, s[84:87], %[dl1] offen offset:2048 sc1\n\t"
      "buffer_load_dword v28, v42, s[84:87], %[dl1] offen offset:3072 sc1\n\t"
      "s_branch 8b\n\t"
      "6:\n\t"
      "s_waitcnt lgkmcnt(0)\n\t"
      "v_mfma_f32_16x16x32_bf16 v[192:195], v[32:35], v[16:19], v[192:195]\n\t"
      "v_add_f32 v100, v100, v124\n\t"
      "v_add_f32 v101, v101, v125\n\t"
      "v_mfma_f32_16x16x32_bf16 v[192:195], v[32:35], v[0:3], v[192:195]\n\t"
      "v_add_f32 v102, v102, v126\n\t"
      "v_add_f32 v103, v103, v127\n\t"
      "v_mfma_f32_16x16x32_bf16 v[204:207], v[36:39], v[16:19], v[204:207]\n\t"
      "v_add_f32 v104, v104, v128\n\t"
      "v_add_f32 v105, v105, v129\n\t"
      "v_mfma_f32_16x16x32_bf16 v[204:207], v[36:39], v[0:3], v[204:207]\n\t"
      "v_add_f32 v106, v106, v130\n\t"
      "v_add_f32 v107, v107, v131\n\t"
      "v_mfma_f32_16x16x32_bf16 v[196:199], v[32:35], v[20:23], v[196:199]\n\t"
      "v_add_f32 v108, v108, v132\n\t"
      "v_add_f32 v109, v109, v133\n\t"
      "v_mfma_f32_16x16x32_bf16 v[196:199], v[32:35], v[4:7], v[196:199]\n\t"
      "v_add_f32 v110, v110, v134\n\t"
      "v_add_f32 v111, v111, v135\n\t"
      "v_mfma_f32_16x16x32_bf16 v[208:211], v[36:39], v[20:23], v[208:211]\n\t"
      "v_add_f32 v112, v112, v136\n\t"
      "v_add_f32 v113, v113, v137\n\t"
      "v_mfma_f32_16x16x32_bf16 v[208:211], v[36:39], v[4:7], v[208:211]\n\t"
      "v_add_f32 v114, v114, v138\n\t"
      "v_add_f32 v115, v115, v139\n\t"
      "v_mfma_f32_16x16x32_bf16 v[200:203], v[32:35], v[24:27], v[200:203]\n\t"
      "v_add_f32 v116, v116, v140\n\t"
      "v_add_f32 v117, v117, v141\n\t"
      "v_mfma_f32_16x16x32_bf16 v[200:203], v[32:35], v[8:11], v[200:203]\n\t"
      "v_add_f32 v118, v118, v142\n\t"
      "v_add_f32 v119, v119, v143\n\t"
      "v_mfma_f32_16x16x32_bf16 v[212:215], v[36:39], v[24:27], v[212:215]\n\t"
      "v_add_f32 v120, v120, v144\n\t"
      "v_add_f32 v121, v121, v145\n\t"
      "v_mfma_f32_16x16x32_bf16 v[212:215], v[36:39], v[8:11], v[212:215]\n\t"
      "v_add_f32 v122, v122, v146\n\t"
      "v_add_f32 v123, v123, v147\n\t"
      "s_cmp_lg_u32 %[bump], 0\n\t"
      "s_cbranch_scc1 4f\n\t"
      "v_mov_b32 v96, s82\n\t"
      "v_mov_b32 v97, s83\n\t"
      "v_mov_b32 v98, 0\n\t"
      "s_mov_b64 exec, 1\n\t"
      "global_store_dwordx2 v98, v[96:97], %[epoch] sc1\n\t"
      "s_mov_b64 exec, -1\n\t"
      "4:\n\t"
      "s_nop 3\n\t"
      "v_pk_add_f32 v[192:193], v[192:193], v[100:101]\n\t"
      "v_pk_add_f32 v[194:195], v[194:195], v[102:103]\n\t"
      "v_pk_add_f32 v[204:205], v[204:205], v[104:105]\n\t"
      "v_pk_add_f32 v[206:207], v[206:207], v[106:107]\n\t"
      "v_cvt_pk_bf16_f32 v216, v192, v193\n\t"
      "v_cvt_pk_bf16_f32 v217, v194, v195\n\t"
      "v_cvt_pk_bf16_f32 v218, v204, v205\n\t"
      "v_cvt_pk_bf16_f32 v219, v206, v207\n\t"
      "s_nop 1\n\t"
      "v_permlane16_swap_b32 v216, v218\n\t"
      "v_permlane16_swap_b32 v217, v219\n\t"
      "ds_bpermute_b32 v68, v51, v216\n\t"
      "ds_bpermute_b32 v69, v51, v217\n\t"
      "ds_bpermute_b32 v70, v51, v218\n\t"
      "ds_bpermute_b32 v71, v51, v219\n\t"
      "v_pk_add_f32 v[196:197], v[196:197], v[108:109]\n\t"
      "v_pk_add_f32 v[198:199], v[198:199], v[110:111]\n\t"
      "v_pk_add_f32 v[208:209], v[208:209], v[112:113]\n\t"
      "v_pk_add_f32 v[210:211], v[210:211], v[114:115]\n\t"
      "v_cvt_pk_bf16_f32 v220, v196, v197\n\t"
      "v_cvt_pk_bf16_f32 v221, v198, v199\n\t"
      "v_cvt_pk_bf16_f32 v222, v208, v209\n\t"
      "v_cvt_pk_bf16_f32 v223, v210, v211\n\t"
      "s_nop 1\n\t"
      "v_permlane16_swap_b32 v220, v222\n\t"
      "v_permlane16_swap_b32 v221, v223\n\t"
      "ds_bpermute_b32 v72, v51, v220\n\t"
      "ds_bpermute_b32 v73, v51, v221\n\t"
      "ds_bpermute_b32 v74, v51, v222\n\t"
      "ds_bpermute_b32 v75, v51, v223\n\t"
      "v_pk_add_f32 v[200:201], v[200:201], v[116:117]\n\t"
      "v_pk_add_f32 v[202:203], v[202:203], v[118:119]\n\t"
      "v_pk_add_f32 v[212:213], v[212:213], v[120:121]\n\t"
      "v_pk_add_f32 v[214:215], v[214:215], v[122:123]\n\t"
      "v_cvt_pk_bf16_f32 v224, v200, v201\n\t"
      "v_cvt_pk_bf16_f32 v225, v202, v203\n\t"
      "v_cvt_pk_bf16_f32 v226, v212, v213\n\t"
      "v_cvt_pk_bf16_f32 v227, v214, v215\n\t"
      "s_nop 1\n\t"
      "v_permlane16_swap_b32 v224, v226\n\t"
      "v_permlane16_swap_b32 v225, v227\n\t"
      "ds_bpermute_b32 v76, v51, v224\n\t"
      "ds_bpermute_b32 v77, v51, v225\n\t"
      "ds_bpermute_b32 v78, v51, v226\n\t"
      "ds_bpermute_b32 v79, v51, v227\n\t"
      "s_waitcnt lgkmcnt(8)\n\t"
      "global_store_dwordx4 v50, v[68:71], s[80:81]\n\t"
      "s_add_u32 s80, s80, 28672\n\t"
      "s_addc_u32 s81, s81, 0\n\t"
      "s_waitcnt lgkmcnt(4)\n\t"
      "global_store_dwordx4 v50, v[72:75], s[80:81]\n\t"
      "s_add_u32 s80, s80, 28672\n\t"
      "s_addc_u32 s81, s81, 0\n\t"
      "s_waitcnt lgkmcnt(0)\n\t"
      "global_store_dwordx4 v50, v[76:79], s[80:81]\n\t"
      "9:\n\t"
      "fa_endc:\n\t"
      :
      : [a1] "s"(c_a1), [qimg] "s"(c_qimg), [qrdy] "s"(c_qrdy), [wop] "s"(c_wop), [pfd] "s"(c_pfd), [a2] "s"(c_a2), [oimg] "s"(c_oimg), [part] "s"(c_part), [po] "s"(c_po), [pb] "s"(c_pb), [own1] "s"(c_own), [bsw] "s"(c_bsw), [gw] "s"(c_gw), [wot] "s"(c_wot), [bump] "s"(c_bump), [epoch] "s"(c_epoch), [dl0] "s"(c_dl0), [dl1] "s"(c_dl1), [sums] "s"(c_sums), [srdy] "s"(c_srdy), [outp] "s"(c_outp)
      : "v0", "v1", "v2", "v3", "v4", "v5", "v6", "v7", "v8", "v9", "v10", "v11", "v12", "v13", "v14", "v15", "v16", "v17", "v18", "v19", "v20", "v21", "v22", "v23", "v24", "v25", "v26", "v27", "v28", "v29", "v30", "v31", "v32", "v33", "v34", "v35", "v36", "v37", "v38", "v39", "v40", "v41", "v42", "v43", "v44", "v45", "v46", "v47", "v48", "v49", "v50", "v51", "v52", "v53", "v54", "v55", "v56", "v57", "v58", "v59", "v60", "v61", "v62", "v63", "v64", "v65", "v66", "v67", "v68", "v69", "v70", "v71", "v72", "v73", "v74", "v75", "v76", "v77", "v78", "v79", "v80", "v81", "v82", "v83", "v84", "v85", "v86", "v87", "v88", "v89", "v90", "v91", "v92", "v93", "v94", "v95", "v96", "v97", "v98", "v99", "v100", "v101", "v102", "v103", "v104", "v105", "v106", "v107", "v108", "v109", "v110", "v111", "v112", "v113", "v114", "v115", "v116", "v117", "v118", "v119", "v120", "v121", "v122", "v123", "v124", "v125", "v126", "v127", "v128", "v129", "v130", "v131", "v132", "v133", "v134", "v135", "v136", "v137", "v138", "v139", "v140", "v141", "v142", "v143", "v144", "v145", "v146", "v147", "v148", "v149", "v150", "v151", "v152", "v153", "v154", "v155", "v156", "v157", "v158", "v159", "v160", "v161", "v162", "v163", "v164", "v165", "v166", "v167", "v168", "v169", "v170", "v171", "v172", "v173", "v174", "v175", "v176", "v177", "v178", "v179", "v180", "v181", "v182", "v183", "v184", "v185", "v186", "v187", "v188", "v189", "v190", "v191", "v192", "v193", "v194", "v195", "v196", "v197", "v198", "v199", "v200", "v201", "v202", "v203", "v204", "v205", "v206", "v207", "v208", "v209", "v210", "v211", "v212", "v213", "v214", "v215", "v216", "v217", "v218", "v219", "v220", "v221", "v222", "v223", "v224", "v225", "v226", "v227", "v228", "v229", "v230", "v231", "v232", "v233", "v234", "v235", "v236", "v237", "v238", "v239", "v240", "v241", "v242", "v243", "v244", "v245", "v246", "v247", "v248", "v249", "v250", "v251", "v252", "v253", "v254", "v255", "s73", "s74", "s75", "s76", "s77", "s78", "s79", "s80", "s81", "s82", "s83", "s84", "s85", "s86", "s87", "s88", "s89", "s90", "s91", "s92", "s93", "s94", "s95", "scc", "vcc", "memory");
  }
}

extern "C" void kernel_launch(void* const* d_in, const int* in_sizes, int n_in, void* d_out, int out_size, void* d_ws,
                              size_t ws_size, hipStream_t stream) {
  Args a;
  for (int i = 0; i < 2; ++i) {
    a.x[i] = (const char*)d_in[5 * i + 0];
    a.k[i] = (const char*)d_in[5 * i + 1];
    a.v[i] = (const char*)d_in[5 * i + 2];
    a.wq[i] = (const char*)d_in[5 * i + 3];
    a.wo[i] = (const char*)d_in[5 * i + 4];
  }
  a.out = (char*)d_out;
  a.ws = (char*)d_ws;
  (void)hipFuncSetAttribute((const void*)fused_attn_kernel, hipFuncAttributeMaxDynamicSharedMemorySize, LDS_BYTES);
  fused_attn_kernel<<<256, 512, LDS_BYTES, stream>>>(a);
}
```
